```python
import torch

def _softmax(x, axis=-1):
    return torch.softmax(x, dim=axis)
def _gelu(x):
    return torch.nn.functional.gelu(x, approximate="tanh")
def _silu(x):
    return torch.nn.functional.silu(x)
def _sigmoid(x):
    return torch.sigmoid(x)
def _layernorm(x, axis=-1, eps=1e-5):
    m = x.mean(dim=axis, keepdim=True)
    v = x.var(dim=axis, keepdim=True, unbiased=False)
    return (x - m) / torch.sqrt(v + eps)
def _scatter_add(tgt, idx, src, axis=0):
    return tgt.clone().index_add_(axis, idx, src)
def _segment_sum(src, seg, n):
    out = torch.zeros((n,) + src.shape[1:], dtype=src.dtype)
    return out.index_add_(0, seg, src)
def _pad(x, pw):
    flat = tuple(v for pair in reversed(pw) for v in pair)
    return torch.nn.functional.pad(x, flat)

kernel_name = 'interleave_layers__linear_kernel_hedgehog__dense_activation_gelu__prompt'

def setup_inputs():

    _w = (lambda a: (lambda t: t.to(torch.bfloat16) if t.is_floating_point() else t)(torch.as_tensor(a)))
    def _A_setup():
        B, S, D, H = 1, 2048, 448, 16
        Dh = D // H
        return {
            "x": _w((lambda *s: torch.randn(*s, dtype=torch.bfloat16) * 0.05)(B, S, D)),
            "wqkv": _w((lambda *s: torch.randn(*s, dtype=torch.bfloat16) * 0.4)(D, 3 * D)),
            "w_phi": _w((lambda *s: torch.randn(*s, dtype=torch.bfloat16) * 0.4)(H, Dh, Dh) * (Dh ** -0.5)),
            "wo": _w((lambda *s: torch.randn(*s, dtype=torch.bfloat16) * 0.05)(D, D)),
        }
    _A_in = _A_setup()

    def _B_setup():
        B, S, D, F = 1, 2048, 448, 1792
        return {
            "x": _w((lambda *s: torch.randn(*s, dtype=torch.bfloat16) * 0.05)(B, S, D)),
            "w1": _w((lambda *s: torch.randn(*s, dtype=torch.bfloat16) * 1.6)(D, F) * (D ** -0.5)),
            "b1": _w((lambda *s: torch.randn(*s, dtype=torch.bfloat16) * 1.6)(F) * 0.01),
            "w2": _w((lambda *s: torch.randn(*s, dtype=torch.bfloat16) * 1.6)(F, D) * (F ** -0.5)),
            "b2": _w((lambda *s: torch.randn(*s, dtype=torch.bfloat16) * 1.6)(D) * 0.01),
        }
    _B_in = _B_setup()

    return {
        "A_x": _A_in["x"],
        "A_wqkv": _A_in["wqkv"],
        "A_w_phi": _A_in["w_phi"],
        "A_wo": _A_in["wo"],
        "B_x": _B_in["x"],
        "B_w1": _B_in["w1"],
        "B_b1": _B_in["b1"],
        "B_w2": _B_in["w2"],
        "B_b2": _B_in["b2"],
    }

def reference(A_x, A_wqkv, A_w_phi, A_wo, B_x, B_w1, B_b1, B_w2, B_b2):
    def _A_ref(x, wqkv, w_phi, wo):
        B, S, D = x.shape
        H = 16; Dh = D // H
        qkv = (lambda x, *d: x.permute(*d))((x @ wqkv).reshape(B, S, 3, H, Dh), 2, 0, 3, 1, 4)
        q, k, v = qkv[0], qkv[1], qkv[2]
        pq = torch.einsum("bhsd,hdf->bhsf", q, w_phi)
        pk = torch.einsum("bhsd,hdf->bhsf", k, w_phi)
        phi_q = torch.concatenate([_softmax(pq, axis=-1), _softmax(-pq, axis=-1)], axis=-1)
        phi_k = torch.concatenate([_softmax(pk, axis=-1), _softmax(-pk, axis=-1)], axis=-1)
        kv = torch.einsum("bhsf,bhsd->bhfd", phi_k, v)
        z  = torch.einsum("bhsf->bhf", phi_k)
        num = torch.einsum("bhqf,bhfd->bhqd", phi_q, kv)
        den = torch.einsum("bhqf,bhf->bhq", phi_q, z)[..., None] + 1e-6
        o = num / den
        return (lambda x, *d: x.permute(*d))(o, 0, 2, 1, 3).reshape(B, S, D) @ wo

    def _B_ref(x, w1, b1, w2, b2):
        h = torch.einsum("bsd,df->bsf", x, w1) + b1
        h = _gelu(h)
        return torch.einsum("bsf,fd->bsd", h, w2) + b2

    x = A_x
    x = x + _A_ref(_layernorm(x, axis=-1), A_wqkv, A_w_phi, A_wo)
    x = x + _B_ref(_layernorm(x, axis=-1), B_w1, B_b1, B_w2, B_b2)
    x = x + _A_ref(_layernorm(x, axis=-1), A_wqkv, A_w_phi, A_wo)
    x = x + _B_ref(_layernorm(x, axis=-1), B_w1, B_b1, B_w2, B_b2)
    return x
```

```cpp
#include <hip/hip_runtime.h>
#include <hip/hip_bf16.h>
#include <stdint.h>

typedef __attribute__((ext_vector_type(2))) __bf16 bf16x2;
typedef __attribute__((ext_vector_type(4))) __bf16 bf16x4;
typedef __attribute__((ext_vector_type(8))) __bf16 bf16x8;
typedef __attribute__((ext_vector_type(16))) float f32x16;
typedef __attribute__((ext_vector_type(4))) float f32x4;
typedef __attribute__((ext_vector_type(2))) unsigned u32x2;
typedef __attribute__((ext_vector_type(4))) unsigned u32x4;
typedef unsigned long long u64;
#define LAS __attribute__((address_space(3)))
#define GAS __attribute__((address_space(1)))
#define DI __device__ __forceinline__
#define MFMA(a, b, c) __builtin_amdgcn_mfma_f32_32x32x16_bf16(__builtin_bit_cast(bf16x8, (a)), __builtin_bit_cast(bf16x8, (b)), (c), 0, 0, 0)

#define WS_EPOCH 0u
#define WS_FLAGS 4096u
#define WS_KVP   65536u
#define WS_O     4259840u
#define WS_X1    7929856u
#define WS_X2    11599872u
#define WS_H     13434880u
#define WS_END   28114944u

#define L_W     0u
#define L_SCR   114688u
#define L_WPHI  147456u
#define L_ID    149504u
#define L_CS    151552u
#define L_B1    152448u
#define L_B2    152960u
#define L_END   153088u

#define SPIN_CAP (1u << 22)

DI unsigned pk2(float a, float b) { bf16x2 v; v[0] = (__bf16)a; v[1] = (__bf16)b; return __builtin_bit_cast(unsigned, v); }
DI float rb(float a) { return (float)(__bf16)a; }
DI float bflo(unsigned u) { return __builtin_bit_cast(float, u << 16); }
DI float bfhi(unsigned u) { return __builtin_bit_cast(float, u & 0xffff0000u); }
DI float xadd(float a) { auto r = __builtin_amdgcn_permlane32_swap(__builtin_bit_cast(unsigned, a), __builtin_bit_cast(unsigned, a), false, false);
                         return __builtin_bit_cast(float, (unsigned)r[0]) + __builtin_bit_cast(float, (unsigned)r[1]); }
DI float xmax(float a) { auto r = __builtin_amdgcn_permlane32_swap(__builtin_bit_cast(unsigned, a), __builtin_bit_cast(unsigned, a), false, false);
                         return fmaxf(__builtin_bit_cast(float, (unsigned)r[0]), __builtin_bit_cast(float, (unsigned)r[1])); }
DI float xmin(float a) { auto r = __builtin_amdgcn_permlane32_swap(__builtin_bit_cast(unsigned, a), __builtin_bit_cast(unsigned, a), false, false);
                         return fminf(__builtin_bit_cast(float, (unsigned)r[0]), __builtin_bit_cast(float, (unsigned)r[1])); }

#define BAR() asm volatile("s_waitcnt lgkmcnt(0)\n\ts_barrier" ::: "memory")
#define DRAIN() asm volatile("s_waitcnt vmcnt(0)" ::: "memory")

DI bf16x4 trr(unsigned addr) { return __builtin_amdgcn_ds_read_tr16_b64_v4bf16((LAS bf16x4*)addr); }
DI u32x4 lds128(unsigned addr) { return *(LAS u32x4*)addr; }
DI void sts128(unsigned addr, u32x4 v) { *(LAS u32x4*)addr = v; }
DI void sts64(unsigned addr, u32x2 v) { *(LAS u32x2*)addr = v; }

DI unsigned pi_x(unsigned k) {
  const unsigned j = k / 28u, r = k - 28u * j;
  const unsigned i = j >> 1, odd = j & 1u, gq = r >> 2, q = gq >> 1, h = gq & 1u;
  unsigned u, slot;
  if (h == 1u && q == 2u) { u = 7u * i + 3u; slot = odd; }
  else { u = 7u * i + 4u * odd + (q >= 2u ? 2u : h); slot = q & 1u; }
  return 8u * u + 4u * slot + (r & 3u);
}
constexpr int unit_feat(int u, int slot) {
  const int i = u / 7, v = u % 7;
  if (v == 3) return 28 * (2 * i + slot) + 20;
  const int j = 2 * i + (v > 3 ? 1 : 0), vv = v > 3 ? v - 4 : v;
  return 28 * j + (vv == 0 ? 8 * slot : (vv == 1 ? 8 * slot + 4 : 16 + 8 * slot));
}
DI unsigned pi_h(unsigned k) { return (k & ~12u) | ((k & 4u) << 1) | ((k & 8u) >> 1); }

template <int NS>
DI f32x16 gemm_tile(unsigned wb, const u32x4 (&xf)[NS], f32x16 acc) {
#pragma unroll
  for (int s = 0; s < NS; ++s) {
    bf16x4 a = trr(wb + s * 1024), b = trr(wb + s * 1024 + 256);
    bf16x8 wf = __builtin_shufflevector(a, b, 0, 1, 2, 3, 4, 5, 6, 7);
    acc = MFMA(wf, xf[s], acc);
  }
  return acc;
}

DI f32x16 zero16() { f32x16 z;
#pragma unroll
  for (int i = 0; i < 16; ++i) z[i] = 0.f;
  return z; }

DI void pack16(const f32x16& v, u32x4& f0, u32x4& f1) {
#pragma unroll
  for (int i = 0; i < 4; ++i) { f0[i] = pk2(v[2 * i], v[2 * i + 1]); f1[i] = pk2(v[8 + 2 * i], v[8 + 2 * i + 1]); }
}

DI void dual_softmax(f32x16 p, bool hh, u32x4& p0, u32x4& p1, u32x4& m0, u32x4& m1) {
#pragma unroll
  for (int i = 0; i < 16; ++i) p[i] = rb(p[i]);
#pragma unroll
  for (int i = 12; i < 16; ++i) p[i] = hh ? p[0] : p[i];
  float mx = p[0], mn = p[0];
#pragma unroll
  for (int i = 1; i < 16; ++i) { mx = fmaxf(mx, p[i]); mn = fminf(mn, p[i]); }
  mx = xmax(mx); mn = xmin(mn);
  const float L = 1.4426950408889634f;
  const float mxl = mx * L, mnl = mn * L;
  f32x16 ep, em; float sp = 0.f, sm = 0.f;
#pragma unroll
  for (int i = 0; i < 16; ++i) {
    float a = __builtin_amdgcn_exp2f(p[i] * L - mxl);
    float b = __builtin_amdgcn_exp2f(mnl - p[i] * L);
    if (i >= 12) { a = hh ? 0.f : a; b = hh ? 0.f : b; }
    ep[i] = a; em[i] = b; sp += a; sm += b;
  }
  sp = xadd(sp); sm = xadd(sm);
  const float rp = 1.0f / sp, rm = 1.0f / sm;
#pragma unroll
  for (int i = 0; i < 16; ++i) { ep[i] *= rp; em[i] *= rm; }
  pack16(ep, p0, p1); pack16(em, m0, m1);
}

struct Params {
  const void* x; const void* wqkv; const void* wphi; const void* wo;
  const void* w1; const void* b1; const void* w2; const void* b2;
  void* out; void* ws;
};

DI void wait_flags(const GAS u64* f, u64 fv, int lane) {
  const GAS u64* p = f + (lane & 15);
  for (unsigned sp = 0; sp < SPIN_CAP; ++sp) {
    u64 v = __hip_atomic_load(p, __ATOMIC_RELAXED, __HIP_MEMORY_SCOPE_AGENT);
    if (__all(v == fv)) break;
    __builtin_amdgcn_s_sleep(1);
  }
}

__global__ void __launch_bounds__(256, 1) fwd(Params P) {
  __shared__ __attribute__((aligned(16))) char smem[L_END];
  const unsigned lb = (unsigned)(size_t)(LAS char*)smem;
  const int tid = threadIdx.x, lane = tid & 63, r = lane & 31;
  const bool hh = lane >= 32;
  const int w = __builtin_amdgcn_readfirstlane(tid >> 6);
  const int b = blockIdx.x;
  const int t = (b & 7) * 2 + (b >> 7), j = (b >> 3) & 15;
  const int g = t * 4 + w;

  GAS char* wsg = (GAS char*)P.ws;
  const u64 E = __hip_atomic_load((GAS u64*)(wsg + WS_EPOCH), __ATOMIC_RELAXED, __HIP_MEMORY_SCOPE_AGENT);
  const u64 FV = (E + 1ull) * 0x9E3779B97F4A7C15ull + 0x632BE59BD9B4E019ull;

  auto rs_ws = __builtin_amdgcn_make_buffer_rsrc(P.ws, 0, WS_END, 0x00020000);
  auto rs_x = __builtin_amdgcn_make_buffer_rsrc((void*)P.x, 0, 2048 * 448 * 2, 0x00020000);
  auto rs_wqkv = __builtin_amdgcn_make_buffer_rsrc((void*)P.wqkv, 0, 448 * 1344 * 2, 0x00020000);
  auto rs_wo = __builtin_amdgcn_make_buffer_rsrc((void*)P.wo, 0, 448 * 448 * 2, 0x00020000);
  auto rs_w1 = __builtin_amdgcn_make_buffer_rsrc((void*)P.w1, 0, 448 * 1792 * 2, 0x00020000);
  auto rs_w2 = __builtin_amdgcn_make_buffer_rsrc((void*)P.w2, 0, 1792 * 448 * 2, 0x00020000);
  auto rs_out = __builtin_amdgcn_make_buffer_rsrc(P.out, 0, 2048 * 448 * 2, 0x00020000);

  const unsigned trb = lb + (unsigned)((8 * (lane >> 5) + ((lane & 15) >> 2)) * 64 + 32 * ((lane >> 4) & 1) + 8 * (lane & 3));
#define FRESH8 int ln_ = lane; asm volatile("" : "+v"(ln_)); const int c8 = ln_ & 7, c8c = c8 < 7 ? c8 : 6, r8 = ln_ >> 3; (void)c8c;
#define FRESH16 int ln_ = lane; asm volatile("" : "+v"(ln_)); const int c16 = ln_ & 15, c16c = c16 < 14 ? c16 : 13, r16 = ln_ >> 4; (void)c16c;
  const unsigned lane16 = (unsigned)lane * 16u;

  u32x2 pf[56];
  u32x4 xf[28];

  auto load_SA = [&]() {
    FRESH8
    const unsigned vq = (unsigned)(((8 * w + r8) * 1344 + 28 * j + 4 * c8c) * 2);
    const unsigned vo = (unsigned)(((8 * w + r8) * 448 + 28 * j + 4 * c8c) * 2);
#pragma unroll
    for (int n = 0; n < 42; ++n) {
      const int tile = n / 14, m = n % 14;
      pf[n] = __builtin_amdgcn_raw_buffer_load_b64(rs_wqkv, vq, (unsigned)((m * 32 * 1344 + 448 * tile) * 2), 0);
    }
#pragma unroll
    for (int m = 0; m < 14; ++m) pf[42 + m] = __builtin_amdgcn_raw_buffer_load_b64(rs_wo, vo, (unsigned)(m * 32 * 448 * 2), 0);
  };
  auto store_SA = [&]() {
    FRESH8
#pragma unroll
    for (int m = 0; m < 14; ++m) {
      const unsigned row = pi_x((unsigned)(32 * m + 8 * w + r8));
      const unsigned a = lb + L_W + row * 64u + (unsigned)c8 * 8u;
#pragma unroll
      for (int tile = 0; tile < 4; ++tile) {
        u32x2 v = pf[tile * 14 + m];
        u32x2 pad; pad[0] = (tile == 1) ? 0x00003F80u : 0u; pad[1] = 0u;
        v = (c8 == 7) ? pad : v;
        sts64(a + tile * 28672, v);
      }
    }
  };
  auto load_SB = [&]() {
    FRESH16
    const unsigned v1 = (unsigned)(((4 * w + r16) * 1792 + 112 * j + 8 * c16c) * 2);
#pragma unroll
    for (int n = 0; n < 28; ++n) {
      u32x4 v = __builtin_amdgcn_raw_buffer_load_b128(rs_w1, v1, (unsigned)(n * 16 * 1792 * 2), 0);
      pf[2 * n][0] = v[0]; pf[2 * n][1] = v[1]; pf[2 * n + 1][0] = v[2]; pf[2 * n + 1][1] = v[3];
    }
  };
  auto store_SB = [&]() {
    FRESH16
#pragma unroll
    for (int n = 0; n < 28; ++n) {
      const unsigned row = pi_x((unsigned)(16 * n + 4 * w + r16));
      const unsigned a = lb + L_W + (unsigned)(c16 >> 2) * 28672u + row * 64u + (unsigned)(c16 & 3) * 16u;
      u32x4 v; v[0] = pf[2 * n][0]; v[1] = pf[2 * n][1]; v[2] = pf[2 * n + 1][0]; v[3] = pf[2 * n + 1][1];
      u32x4 pad; pad[0] = (c16 == 14) ? 0x00003F80u : 0u; pad[1] = 0u; pad[2] = 0u; pad[3] = 0u;
      v = (c16 >= 14) ? pad : v;
      sts128(a, v);
    }
  };
  auto load_SC = [&]() {
    FRESH8
    const unsigned v2 = (unsigned)(((8 * w + r8) * 448 + 28 * j + 4 * c8c) * 2);
#pragma unroll
    for (int n = 0; n < 56; ++n) pf[n] = __builtin_amdgcn_raw_buffer_load_b64(rs_w2, v2, (unsigned)(n * 32 * 448 * 2), 0);
  };
  auto store_SC = [&]() {
    FRESH8
    const unsigned row0 = pi_h((unsigned)(8 * w + r8));
    const unsigned a = lb + L_W + row0 * 64u + (unsigned)c8 * 8u;
#pragma unroll
    for (int n = 0; n < 56; ++n) {
      u32x2 v = pf[n]; u32x2 pad; pad[0] = 0u; pad[1] = 0u;
      v = (c8 == 7) ? pad : v;
      sts64(a + n * 2048, v);
    }
  };
  auto store_units = [&](unsigned base, const u32x2 (&Pq)[4]) {
    const int ub = 7 * (j >> 1) + 4 * (j & 1);
    {
      const int u = ub + (hh ? 1 : 0);
      u32x4 v; v[0] = Pq[0][0]; v[1] = Pq[0][1]; v[2] = Pq[1][0]; v[3] = Pq[1][1];
      __builtin_amdgcn_raw_buffer_store_b128(v, rs_ws, base + (unsigned)((u >> 1) * 1024 + ((u & 1) * 32 + r) * 16), 0, 16);
    }
    if (!hh) {
      const int u = ub + 2;
      u32x4 v; v[0] = Pq[2][0]; v[1] = Pq[2][1]; v[2] = Pq[3][0]; v[3] = Pq[3][1];
      __builtin_amdgcn_raw_buffer_store_b128(v, rs_ws, base + (unsigned)((u >> 1) * 1024 + ((u & 1) * 32 + r) * 16), 0, 16);
    } else {
      const int u = 7 * (j >> 1) + 3;
      __builtin_amdgcn_raw_buffer_store_b64(Pq[2], rs_ws, base + (unsigned)((u >> 1) * 1024 + ((u & 1) * 32 + r) * 16 + 8 * (j & 1)), 0, 16);
    }
  };
  auto load_x28 = [&](unsigned base) {
#pragma unroll
    for (int s = 0; s < 28; ++s) xf[s] = __builtin_amdgcn_raw_buffer_load_b128(rs_ws, base + lane16, (unsigned)(s * 1024), 16);
  };
  auto publish = [&](unsigned fidx) {
    DRAIN(); BAR();
    if (tid == 0) __hip_atomic_store((GAS u64*)(wsg + WS_FLAGS) + fidx, FV, __ATOMIC_RELAXED, __HIP_MEMORY_SCOPE_AGENT);
  };
  auto await = [&](unsigned fidx) {
    if (w == 0) wait_flags((const GAS u64*)(wsg + WS_FLAGS) + fidx, FV, lane);
    BAR();
  };
  auto colsum = [&](int tile, int idx) {
    u32x4 ones; ones[0] = ones[1] = ones[2] = ones[3] = 0x3F803F80u;
    f32x16 a = zero16();
    const unsigned wb = trb + L_W + (unsigned)tile * 28672u;
#pragma unroll
    for (int s = 0; s < 28; ++s) {
      bf16x4 x0 = trr(wb + s * 1024), x1 = trr(wb + s * 1024 + 256);
      bf16x8 wf = __builtin_shufflevector(x0, x1, 0, 1, 2, 3, 4, 5, 6, 7);
      a = MFMA(wf, ones, a);
    }
    if (r == 0) {
#pragma unroll
      for (int q = 0; q < 4; ++q) {
        f32x4 v; v[0] = a[4 * q]; v[1] = a[4 * q + 1]; v[2] = a[4 * q + 2]; v[3] = a[4 * q + 3];
        *(LAS f32x4*)(lb + L_CS + (unsigned)(idx * 128 + (8 * q + 4 * (hh ? 1 : 0)) * 4)) = v;
      }
    }
  };
  auto rowc = [&](unsigned tab) {
    f32x16 c;
#pragma unroll
    for (int q = 0; q < 4; ++q) {
      f32x4 v = *(LAS f32x4*)(lb + tab + (unsigned)((8 * q) * 4) + (hh ? 16u : 0u));
      c[4 * q] = v[0]; c[4 * q + 1] = v[1]; c[4 * q + 2] = v[2]; c[4 * q + 3] = v[3];
    }
    return c;
  };
  auto ssq28 = [&]() {
    float s0 = 0.f, s1 = 0.f;
#pragma unroll
    for (int s = 0; s < 28; ++s) {
#pragma unroll
      for (int i = 0; i < 4; i += 2) {
        bf16x2 a = __builtin_bit_cast(bf16x2, (unsigned)xf[s][i]), c = __builtin_bit_cast(bf16x2, (unsigned)xf[s][i + 1]);
        s0 = __builtin_amdgcn_fdot2_f32_bf16(a, a, s0, false);
        s1 = __builtin_amdgcn_fdot2_f32_bf16(c, c, s1, false);
      }
    }
    return xadd(s0 + s1);
  };
  auto ln_stats = [&](float sum, float ssq, float& mb, float& rs) {
    const float mean = sum * (1.0f / 448.0f);
    float var = ssq * (1.0f / 448.0f) - mean * mean;
    var = fmaxf(var, 0.f);
    mb = rb(mean);
    const float sb = rb(__builtin_sqrtf(rb(rb(var) + 1e-5f)));
    rs = 1.0f / sb;
  };

  load_SA();
  {
    const unsigned rowb = (unsigned)((g * 32 + r) * 448 * 2);
#pragma unroll
    for (int s = 0; s < 28; ++s) {
      const unsigned o0 = hh ? (unsigned)(unit_feat(2 * s + 1, 0) * 2) : (unsigned)(unit_feat(2 * s, 0) * 2);
      const unsigned o1 = hh ? (unsigned)(unit_feat(2 * s + 1, 1) * 2) : (unsigned)(unit_feat(2 * s, 1) * 2);
      u32x2 a = __builtin_amdgcn_raw_buffer_load_b64(rs_x, rowb + o0, 0, 0);
      u32x2 c = __builtin_amdgcn_raw_buffer_load_b64(rs_x, rowb + o1, 0, 0);
      xf[s][0] = a[0]; xf[s][1] = a[1]; xf[s][2] = c[0]; xf[s][3] = c[1];
    }
  }
  u32x2 xr[4];
  {
    const unsigned rowb = (unsigned)(((g * 32 + r) * 448 + 28 * j + (hh ? 4 : 0)) * 2);
#pragma unroll
    for (int q = 0; q < 4; ++q) xr[q] = __builtin_amdgcn_raw_buffer_load_b64(rs_x, rowb, (unsigned)(16 * q), 0);
  }
  if (w == 0) {
    const __hip_bfloat16* wp = (const __hip_bfloat16*)P.wphi + j * 784;
#pragma unroll
    for (int i = 0; i < 2; ++i) {
      u32x4 v;
#pragma unroll
      for (int e = 0; e < 8; e += 2) {
        const int d0 = 16 * i + 8 * (e >> 2) + (hh ? 4 : 0) + (e & 3);
        unsigned lo = (d0 < 28 && r < 28) ? (unsigned)*(const unsigned short*)(wp + d0 * 28 + r) : 0u;
        unsigned hi = (d0 + 1 < 28 && r < 28) ? (unsigned)*(const unsigned short*)(wp + (d0 + 1) * 28 + r) : 0u;
        v[e >> 1] = lo | (hi << 16);
      }
      sts128(lb + L_WPHI + (unsigned)i * 1024u + lane16, v);
    }
  } else if (w == 1) {
#pragma unroll
    for (int i = 0; i < 2; ++i) {
      u32x4 v;
#pragma unroll
      for (int e = 0; e < 8; e += 2) {
        const int d0 = 16 * i + 8 * (e >> 2) + (hh ? 4 : 0) + (e & 3);
        v[e >> 1] = (d0 == r ? 0x3F80u : 0u) | (d0 + 1 == r ? 0x3F800000u : 0u);
      }
      sts128(lb + L_ID + (unsigned)i * 1024u + lane16, v);
    }
  } else if (w == 2) {
    const __hip_bfloat16* bp = (const __hip_bfloat16*)P.b1 + 112 * j;
#pragma unroll
    for (int i = 0; i < 2; ++i) {
      const int n = lane + 64 * i;
      float v = n < 112 ? bflo((unsigned)*(const unsigned short*)(bp + n)) : 0.f;
      *(LAS float*)(lb + L_B1 + (unsigned)n * 4u) = v;
    }
  } else {
    const __hip_bfloat16* bp = (const __hip_bfloat16*)P.b2 + 28 * j;
    if (lane < 32) {
      float v = lane < 28 ? bflo((unsigned)*(const unsigned short*)(bp + lane)) : 0.f;
      *(LAS float*)(lb + L_B2 + (unsigned)lane * 4u) = v;
    }
  }
  store_SA();
  BAR();
  if (w < 3) colsum(w, w);

  for (int layer = 0; layer < 2; ++layer) {
    const unsigned fb = (unsigned)layer * 5u * 256u;
    if (layer == 1) {
      await(4u * 256u + (unsigned)t * 16u);
      load_x28(WS_X2 + (unsigned)g * 28672u);
    }
    load_SB();
    u32x4 pq_p0, pq_p1, pq_m0, pq_m1;
    float mb, rs;
    {
      f32x16 ak = gemm_tile<28>(trb + L_W + 28672u, xf, zero16());
      const float ssq = ssq28();
      BAR();
      const float sum = xadd(hh ? ak[12] : 0.f);
      ln_stats(sum, ssq, mb, rs);
      u32x4 kf0, kf1;
      {
        f32x16 cs = rowc(L_CS + 128u);
#pragma unroll
        for (int i = 0; i < 16; ++i) ak[i] = (ak[i] - mb * cs[i]) * rs;
        pack16(ak, kf0, kf1);
      }
      f32x16 av = gemm_tile<28>(trb + L_W + 57344u, xf, zero16());
      const u32x4 wp0 = lds128(lb + L_WPHI + lane16), wp1 = lds128(lb + L_WPHI + 1024u + lane16);
      const u32x4 id0 = lds128(lb + L_ID + lane16), id1 = lds128(lb + L_ID + 1024u + lane16);
      f32x16 pk = MFMA(wp0, kf0, zero16());
      pk = MFMA(wp1, kf1, pk);
      u32x4 kp0, kp1, km0, km1;
      dual_softmax(pk, hh, kp0, kp1, km0, km1);
      u32x4 vf0, vf1;
      {
        f32x16 cs = rowc(L_CS + 256u);
#pragma unroll
        for (int i = 0; i < 16; ++i) av[i] = (av[i] - mb * cs[i]) * rs;
        pack16(av, vf0, vf1);
      }
      f32x16 tp = MFMA(kp0, id0, zero16()); tp = MFMA(kp1, id1, tp);
      f32x16 tm = MFMA(km0, id0, zero16()); tm = MFMA(km1, id1, tm);
      f32x16 tv = MFMA(vf0, id0, zero16()); tv = MFMA(vf1, id1, tv);
#pragma unroll
      for (int i = 0; i < 16; ++i) tv[i] = (r == 28) ? 1.0f : (r > 28 ? 0.f : tv[i]);
      u32x4 tp0, tp1, tm0, tm1, tv0, tv1;
      pack16(tp, tp0, tp1); pack16(tm, tm0, tm1); pack16(tv, tv0, tv1);
      f32x16 kvp = MFMA(tp0, tv0, zero16()); kvp = MFMA(tp1, tv1, kvp);
      f32x16 kvm = MFMA(tm0, tv0, zero16()); kvm = MFMA(tm1, tv1, kvm);
      {
        const unsigned a = lb + L_SCR + (unsigned)w * 8192u + lane16;
#pragma unroll
        for (int q = 0; q < 4; ++q) {
          f32x4 v; v[0] = kvp[4 * q]; v[1] = kvp[4 * q + 1]; v[2] = kvp[4 * q + 2]; v[3] = kvp[4 * q + 3];
          *(LAS f32x4*)(a + q * 1024) = v;
          f32x4 u; u[0] = kvm[4 * q]; u[1] = kvm[4 * q + 1]; u[2] = kvm[4 * q + 2]; u[3] = kvm[4 * q + 3];
          *(LAS f32x4*)(a + 4096 + q * 1024) = u;
        }
      }
      BAR();
      {
        const unsigned a = lb + L_SCR + (unsigned)w * 2048u + lane16;
        const unsigned gb = WS_KVP + (unsigned)((layer * 16 + j) * 16 + t) * 8192u + (unsigned)w * 2048u + lane16;
#pragma unroll
        for (int q = 0; q < 2; ++q) {
          f32x4 s = *(LAS f32x4*)(a + q * 1024);
#pragma unroll
          for (int ww = 1; ww < 4; ++ww) s += *(LAS f32x4*)(a + q * 1024 + ww * 8192);
          __builtin_amdgcn_raw_buffer_store_b128(__builtin_bit_cast(u32x4, s), rs_ws, gb + q * 1024, 0, 16);
        }
      }
      publish(fb + 0u * 256u + (unsigned)(j * 16 + t));
      f32x16 aq = gemm_tile<28>(trb + L_W, xf, zero16());
      u32x4 qf0, qf1;
      {
        f32x16 cs = rowc(L_CS);
#pragma unroll
        for (int i = 0; i < 16; ++i) aq[i] = (aq[i] - mb * cs[i]) * rs;
        pack16(aq, qf0, qf1);
      }
      f32x16 pq = MFMA(wp0, qf0, zero16());
      pq = MFMA(wp1, qf1, pq);
      dual_softmax(pq, hh, pq_p0, pq_p1, pq_m0, pq_m1);
    }
    await(fb + 0u * 256u + (unsigned)j * 16u);
    if (layer == 1 && b == 0 && tid == 0)
      __hip_atomic_store((GAS u64*)(wsg + WS_EPOCH), E + 1ull, __ATOMIC_RELAXED, __HIP_MEMORY_SCOPE_AGENT);
    {
      const unsigned gb = WS_KVP + (unsigned)((layer * 16 + j) * 16) * 8192u + (unsigned)w * 2048u + lane16;
      u32x4 pa[16], pb[16];
#pragma unroll
      for (int tt = 0; tt < 16; ++tt) {
        pa[tt] = __builtin_amdgcn_raw_buffer_load_b128(rs_ws, gb, (unsigned)(tt * 8192), 16);
        pb[tt] = __builtin_amdgcn_raw_buffer_load_b128(rs_ws, gb, (unsigned)(tt * 8192 + 1024), 16);
      }
      f32x4 sa = __builtin_bit_cast(f32x4, pa[0]), sb = __builtin_bit_cast(f32x4, pb[0]);
#pragma unroll
      for (int tt = 1; tt < 16; ++tt) { sa += __builtin_bit_cast(f32x4, pa[tt]); sb += __builtin_bit_cast(f32x4, pb[tt]); }
      u32x4 v; v[0] = pk2(sa[0], sa[1]); v[1] = pk2(sa[2], sa[3]); v[2] = pk2(sb[0], sb[1]); v[3] = pk2(sb[2], sb[3]);
      sts128(lb + L_SCR + (unsigned)w * 1024u + lane16, v);
      BAR();
      const u32x4 k0 = lds128(lb + L_SCR + lane16), k1 = lds128(lb + L_SCR + 1024u + lane16);
      const u32x4 k2 = lds128(lb + L_SCR + 2048u + lane16), k3 = lds128(lb + L_SCR + 3072u + lane16);
      f32x16 nu = MFMA(k0, pq_p0, zero16());
      nu = MFMA(k1, pq_p1, nu); nu = MFMA(k2, pq_m0, nu); nu = MFMA(k3, pq_m1, nu);
      float den = xadd(hh ? nu[12] : 0.f);
      den = rb(rb(den) + 1e-6f);
      const float rd = 1.0f / den;
      u32x2 Pq[4];
#pragma unroll
      for (int q = 0; q < 4; ++q) {
        Pq[q][0] = pk2(rb(nu[4 * q]) * rd, rb(nu[4 * q + 1]) * rd);
        Pq[q][1] = pk2(rb(nu[4 * q + 2]) * rd, rb(nu[4 * q + 3]) * rd);
      }
      store_units(WS_O + (unsigned)(layer * 64 + g) * 28672u, Pq);
    }
    publish(fb + 1u * 256u + (unsigned)(t * 16 + j));
    await(fb + 1u * 256u + (unsigned)t * 16u);
    load_x28(WS_O + (unsigned)(layer * 64 + g) * 28672u);
    {
      f32x16 a = gemm_tile<28>(trb + L_W + 86016u, xf, zero16());
#pragma unroll
      for (int q = 0; q < 4; ++q) {
        xr[q][0] = pk2(bflo(xr[q][0]) + rb(a[4 * q]), bfhi(xr[q][0]) + rb(a[4 * q + 1]));
        xr[q][1] = pk2(bflo(xr[q][1]) + rb(a[4 * q + 2]), bfhi(xr[q][1]) + rb(a[4 * q + 3]));
      }
      store_units(WS_X1 + (unsigned)(layer * 64 + g) * 28672u, xr);
    }
    publish(fb + 2u * 256u + (unsigned)(t * 16 + j));
    store_SB();
    await(fb + 2u * 256u + (unsigned)t * 16u);
    load_x28(WS_X1 + (unsigned)(layer * 64 + g) * 28672u);
    if (layer == 0) colsum(w, 3 + w);
    {
      const unsigned hb = WS_H + (unsigned)(layer * 64 + g) * 114688u + (unsigned)(7 * j) * 1024u + lane16;
      f32x16 a3 = gemm_tile<28>(trb + L_W + 3u * 28672u, xf, zero16());
      const float ssq = ssq28();
      load_SC();
      BAR();
      const float sum = xadd(hh ? 0.f : a3[8]);
      ln_stats(sum, ssq, mb, rs);
#pragma unroll
      for (int cc = 0; cc < 4; ++cc) {
        const int c = (cc + 3) & 3;
        f32x16 a;
        if (cc == 0) a = a3; else a = gemm_tile<28>(trb + L_W + (unsigned)c * 28672u, xf, zero16());
        f32x16 cs = rowc(L_CS + (unsigned)(3 + c) * 128u);
        f32x16 bb = rowc(L_B1 + (unsigned)c * 128u);
        const int nr = (c == 3) ? 8 : 16;
#pragma unroll
        for (int i = 0; i < 16; ++i) {
          if (i < nr) {
            float v = rb((a[i] - mb * cs[i]) * rs);
            v = rb(v + bb[i]);
            const float u = v * (1.5957691216057308f * 1.4426950408889634f) * (1.0f + 0.044715f * v * v);
            const float e = __builtin_amdgcn_exp2f(-u);
            a[i] = v * __builtin_amdgcn_rcpf(1.0f + e);
          }
        }
        u32x4 f0, f1;
        pack16(a, f0, f1);
        __builtin_amdgcn_raw_buffer_store_b128(f0, rs_ws, hb + (unsigned)(2 * c) * 1024u, 0, 16);
        if (c != 3) __builtin_amdgcn_raw_buffer_store_b128(f1, rs_ws, hb + (unsigned)(2 * c + 1) * 1024u, 0, 16);
      }
    }
    publish(fb + 3u * 256u + (unsigned)(t * 16 + j));
    store_SC();
    await(fb + 3u * 256u + (unsigned)t * 16u);
    {
      const unsigned hb = WS_H + (unsigned)(layer * 64 + g) * 114688u + lane16;
      u32x4 xg[28];
#pragma unroll
      for (int s = 0; s < 28; ++s) xf[s] = __builtin_amdgcn_raw_buffer_load_b128(rs_ws, hb, (unsigned)(s * 1024), 16);
#pragma unroll
      for (int s = 0; s < 28; ++s) xg[s] = __builtin_amdgcn_raw_buffer_load_b128(rs_ws, hb, (unsigned)((28 + s) * 1024), 16);
      f32x16 a = gemm_tile<28>(trb + L_W, xf, zero16());
#pragma unroll
      for (int s = 0; s < 28; ++s) xf[s] = __builtin_amdgcn_raw_buffer_load_b128(rs_ws, hb, (unsigned)((56 + s) * 1024), 16);
      a = gemm_tile<28>(trb + L_W + 28672u, xg, a);
#pragma unroll
      for (int s = 0; s < 28; ++s) xg[s] = __builtin_amdgcn_raw_buffer_load_b128(rs_ws, hb, (unsigned)((84 + s) * 1024), 16);
      if (layer == 0) load_SA();
      a = gemm_tile<28>(trb + L_W + 2u * 28672u, xf, a);
      a = gemm_tile<28>(trb + L_W + 3u * 28672u, xg, a);
      f32x16 bb = rowc(L_B2);
#pragma unroll
      for (int q = 0; q < 4; ++q) {
        xr[q][0] = pk2(bflo(xr[q][0]) + rb(rb(a[4 * q]) + bb[4 * q]), bfhi(xr[q][0]) + rb(rb(a[4 * q + 1]) + bb[4 * q + 1]));
        xr[q][1] = pk2(bflo(xr[q][1]) + rb(rb(a[4 * q + 2]) + bb[4 * q + 2]), bfhi(xr[q][1]) + rb(rb(a[4 * q + 3]) + bb[4 * q + 3]));
      }
    }
    if (layer == 0) {
      store_units(WS_X2 + (unsigned)g * 28672u, xr);
      publish(4u * 256u + (unsigned)(t * 16 + j));
      store_SA();
    } else {
      const unsigned rowb = (unsigned)(((g * 32 + r) * 448 + 28 * j + (hh ? 4 : 0)) * 2);
#pragma unroll
      for (int q = 0; q < 3; ++q) __builtin_amdgcn_raw_buffer_store_b64(xr[q], rs_out, rowb + 16 * q, 0, 0);
      if (!hh) __builtin_amdgcn_raw_buffer_store_b64(xr[3], rs_out, rowb + 48, 0, 0);
    }
  }
}

extern "C" void kernel_launch(void* const* d_in, const int* in_sizes, int n_in,
                              void* d_out, int out_size, void* d_ws, size_t ws_size,
                              hipStream_t stream) {
  Params p;
  p.x = d_in[0]; p.wqkv = d_in[1]; p.wphi = d_in[2]; p.wo = d_in[3];
  p.w1 = d_in[5]; p.b1 = d_in[6]; p.w2 = d_in[7]; p.b2 = d_in[8];
  p.out = d_out; p.ws = d_ws;
  fwd<<<256, 256, 0, stream>>>(p);
}
```

```cpp
#include <hip/hip_runtime.h>
#include <hip/hip_bf16.h>
#include <stdint.h>
#include <utility>

#if defined(__HIP_DEVICE_COMPILE__)
#pragma clang attribute push(__attribute__((target("no-packed-fp32-ops"))), apply_to = function)
#endif

typedef __attribute__((ext_vector_type(2))) __bf16 bf16x2;
typedef __attribute__((ext_vector_type(8))) __bf16 bf16x8;
typedef __attribute__((ext_vector_type(16))) float f32x16;
typedef __attribute__((ext_vector_type(8))) float f32x8;
typedef __attribute__((ext_vector_type(4))) float f32x4;
typedef __attribute__((ext_vector_type(2))) float f32x2;
typedef __attribute__((ext_vector_type(2))) unsigned u32x2;
typedef __attribute__((ext_vector_type(4))) unsigned u32x4;
typedef unsigned long long u64;
#define LAS __attribute__((address_space(3)))
#define GAS __attribute__((address_space(1)))
#define DI __device__ __forceinline__
#define MFMA(a, b, c) __builtin_amdgcn_mfma_f32_32x32x16_bf16(__builtin_bit_cast(bf16x8, (a)), __builtin_bit_cast(bf16x8, (b)), (c), 0, 0, 0)

#define WS_EPOCH 0u
#define WS_FLAGS 4096u
#define WS_KV    65536u
#define WS_O     4259840u
#define WS_X1    7929856u
#define WS_X2    11599872u
#define WS_H     13434880u
#define WS_WT    28114944u
#define G_WO     78848u
#define G_W1     204288u
#define G_STRIDE 319488u
#define WS_END   (WS_WT + 16u * G_STRIDE)

#define L_K     0u
#define T29     25984u
#define L_R0    78848u
#define SLOT    28672u
#define T28     25088u
#define L_X     136192u
#define L_XS    0u
#define L_WPHI  152576u
#define L_ID    154624u
#define L_CS    156672u
#define L_B1    157568u
#define L_B2    158080u
#define L_ST    158208u
#define L_END   160256u

#define SPIN_CAP (1u << 22)

DI unsigned pk2(float a, float b) { bf16x2 v; v[0] = (__bf16)a; v[1] = (__bf16)b; return __builtin_bit_cast(unsigned, v); }
DI unsigned f2u(float a) { return __builtin_bit_cast(unsigned, a); }
DI float rb(float a) { return (float)(__bf16)a; }
DI float bflo(unsigned u) { return __builtin_bit_cast(float, u << 16); }
DI float bfhi(unsigned u) { return __builtin_bit_cast(float, u & 0xffff0000u); }
DI float xadd(float a) { auto r = __builtin_amdgcn_permlane32_swap(__builtin_bit_cast(unsigned, a), __builtin_bit_cast(unsigned, a), false, false);
                         return __builtin_bit_cast(float, (unsigned)r[0]) + __builtin_bit_cast(float, (unsigned)r[1]); }
DI float xmax(float a) { auto r = __builtin_amdgcn_permlane32_swap(__builtin_bit_cast(unsigned, a), __builtin_bit_cast(unsigned, a), false, false);
                         return fmaxf(__builtin_bit_cast(float, (unsigned)r[0]), __builtin_bit_cast(float, (unsigned)r[1])); }
DI float xmin(float a) { auto r = __builtin_amdgcn_permlane32_swap(__builtin_bit_cast(unsigned, a), __builtin_bit_cast(unsigned, a), false, false);
                         return fminf(__builtin_bit_cast(float, (unsigned)r[0]), __builtin_bit_cast(float, (unsigned)r[1])); }

#define ST() do {} while (0)
#define SCHED() __builtin_amdgcn_sched_barrier(0)
#define BAR() do { SCHED(); asm volatile("s_waitcnt lgkmcnt(0)\n\ts_barrier" ::: "memory"); SCHED(); } while (0)
#define BAR0() do { SCHED(); asm volatile("s_barrier" ::: "memory"); SCHED(); } while (0)
#define DRAIN() asm volatile("s_waitcnt vmcnt(0)" ::: "memory")

typedef __attribute__((ext_vector_type(4))) __bf16 bf16x4;
DI u32x2 trr(unsigned addr) { return __builtin_bit_cast(u32x2, __builtin_amdgcn_ds_read_tr16_b64_v4bf16((LAS bf16x4*)addr)); }
DI u32x4 lds128(unsigned addr) { return *(LAS u32x4*)addr; }
DI void sts128(unsigned addr, u32x4 v) { *(LAS u32x4*)addr = v; }
DI f32x4 ldsf4(unsigned addr) { return *(LAS f32x4*)addr; }
DI void stsf4(unsigned addr, f32x4 v) { *(LAS f32x4*)addr = v; }

template <int I> struct IC { static constexpr int v = I; };
template <class F, int... Is> DI void sfor_impl(F&& f, std::integer_sequence<int, Is...>) { (f(IC<Is>{}), ...); }
template <int N, class F> DI void sfor(F&& f) { sfor_impl(f, std::make_integer_sequence<int, N>{}); }

#define PD 4
template <int NS, int STR, class F>
DI f32x16 gemm_f(unsigned wb, const u32x4* xf, f32x16 acc, F&& f) {
  asm volatile("" : "+v"(wb));
  u32x4 wf[NS];
  SCHED();
#pragma unroll
  for (int s = 0; s < PD; ++s) wf[s] = lds128(wb + s * STR);
  SCHED();
  sfor<NS>([&](auto ic) {
    constexpr int s = decltype(ic)::v;
    if constexpr (s + PD < NS) wf[s + PD] = lds128(wb + (s + PD) * STR);
    acc = MFMA(wf[s], xf[s], acc);
    f(ic);
    SCHED();
  });
  return acc;
}
template <int NS, int STR>
DI f32x16 gemm(unsigned wb, const u32x4* xf, f32x16 acc) { return gemm_f<NS, STR>(wb, xf, acc, [](auto) {}); }

DI f32x16 zero16() { f32x16 z;
#pragma unroll
  for (int i = 0; i < 16; ++i) z[i] = 0.f;
  return z; }

DI void pack16(const f32x16& v, u32x4& f0, u32x4& f1) {
#pragma unroll
  for (int i = 0; i < 4; ++i) { f0[i] = pk2(v[2 * i], v[2 * i + 1]); f1[i] = pk2(v[8 + 2 * i], v[8 + 2 * i + 1]); }
}
DI void st16(unsigned a, const f32x16& v) {
#pragma unroll
  for (int q = 0; q < 4; ++q) { f32x4 x; x[0] = v[4 * q]; x[1] = v[4 * q + 1]; x[2] = v[4 * q + 2]; x[3] = v[4 * q + 3]; stsf4(a + q * 1024, x); }
}
DI void add16(unsigned a, f32x16& v) {
#pragma unroll
  for (int q = 0; q < 4; ++q) { f32x4 x = ldsf4(a + q * 1024); v[4 * q] += x[0]; v[4 * q + 1] += x[1]; v[4 * q + 2] += x[2]; v[4 * q + 3] += x[3]; }
}

DI void dual_softmax(f32x16 p, bool hh, float rs, float os, u32x4& p0, u32x4& p1, u32x4& m0, u32x4& m1) {
#pragma unroll
  for (int i = 12; i < 16; ++i) p[i] = hh ? p[0] : p[i];
  float mx = p[0], mn = p[0];
#pragma unroll
  for (int i = 1; i < 16; ++i) { mx = fmaxf(mx, p[i]); mn = fminf(mn, p[i]); }
  mx = xmax(mx); mn = xmin(mn);
  const float L = 1.4426950408889634f * rs;
  const float mxl = mx * L, mnl = mn * L;
  {
    f32x16 e; float sp = 0.f;
#pragma unroll
    for (int i = 0; i < 16; ++i) {
      float a = __builtin_amdgcn_exp2f(p[i] * L - mxl);
      if (i >= 12) a = hh ? 0.f : a;
      e[i] = a; sp += a;
    }
    const float rp = os * __builtin_amdgcn_rcpf(xadd(sp));
#pragma unroll
    for (int i = 0; i < 16; ++i) e[i] *= rp;
    pack16(e, p0, p1);
  }
  SCHED();
  {
    float sm = 0.f;
#pragma unroll
    for (int i = 0; i < 16; ++i) {
      float c = __builtin_amdgcn_exp2f(mnl - p[i] * L);
      if (i >= 12) c = hh ? 0.f : c;
      p[i] = c; sm += c;
    }
    const float rm = os * __builtin_amdgcn_rcpf(xadd(sm));
#pragma unroll
    for (int i = 0; i < 16; ++i) p[i] *= rm;
    pack16(p, m0, m1);
  }
}

DI void ssq_step(const u32x4& x, float& s0, float& s1) {
#pragma unroll
  for (int i = 0; i < 4; i += 2) {
    bf16x2 a = __builtin_bit_cast(bf16x2, (unsigned)x[i]), c = __builtin_bit_cast(bf16x2, (unsigned)x[i + 1]);
    s0 = __builtin_amdgcn_fdot2_f32_bf16(a, a, s0, false);
    s1 = __builtin_amdgcn_fdot2_f32_bf16(c, c, s1, false);
  }
}

DI constexpr int unit_f0(int u) { const int p = u / 7, i = u - 7 * p; return 56 * p + (int)((0x142C201C100400ull >> (8 * i)) & 0xFFu); }
DI constexpr int unit_f1(int u) { const int p = u / 7, i = u - 7 * p; return 56 * p + (int)((0x30342824180C08ull >> (8 * i)) & 0xFFu); }

struct Params {
  const void* x; const void* wqkv; const void* wphi; const void* wo;
  const void* w1; const void* b1; const void* w2; const void* b2;
  void* out; void* ws;
};

DI void wait_flags(const GAS u64* f, u64 fv, int lane) {
  const GAS u64* p = f + (lane & 15);
#pragma unroll 1
  for (unsigned sp = 0; sp < SPIN_CAP; ++sp) {
    u64 v = __hip_atomic_load(p, __ATOMIC_RELAXED, __HIP_MEMORY_SCOPE_AGENT);
    if (__builtin_amdgcn_ballot_w64(v != fv) == 0ull) break;
    __builtin_amdgcn_s_sleep(1);
  }
}

__global__ void __launch_bounds__(512, 2) fwd(Params P) {
  __shared__ __attribute__((aligned(16))) char smem[L_END];
  const unsigned lb = (unsigned)(size_t)(LAS char*)smem;
  int tid = threadIdx.x, lane = tid & 63, r = lane & 31;
  bool hh = lane >= 32;
  int w = __builtin_amdgcn_readfirstlane(tid >> 6);
  int st = w & 3, kh = w >> 2;
  int b = blockIdx.x;
  int t = (b & 7) * 2 + (b >> 7), j = (b >> 3) & 15;
  int g = t * 4 + st;

  GAS char* wsg = (GAS char*)P.ws;
  u64 E = __hip_atomic_load((GAS u64*)(wsg + WS_EPOCH), __ATOMIC_RELAXED, __HIP_MEMORY_SCOPE_AGENT);
  u64 FV = 0;

  auto rs_ws = __builtin_amdgcn_make_buffer_rsrc(P.ws, 0, WS_END, 0x00020000);
  auto rs_x = __builtin_amdgcn_make_buffer_rsrc((void*)P.x, 0, 2048 * 448 * 2, 0x00020000);
  auto rs_out = __builtin_amdgcn_make_buffer_rsrc(P.out, 0, 2048 * 448 * 2, 0x00020000);
  auto rs_wo = __builtin_amdgcn_make_buffer_rsrc((void*)P.wo, 0, 448 * 448 * 2, 0x00020000);
  auto rs_w1 = __builtin_amdgcn_make_buffer_rsrc((void*)P.w1, 0, 448 * 1792 * 2, 0x00020000);
  auto rs_w2 = __builtin_amdgcn_make_buffer_rsrc((void*)P.w2, 0, 1792 * 448 * 2, 0x00020000);
  auto rs_wq = __builtin_amdgcn_make_buffer_rsrc((void*)P.wqkv, 0, 448 * 1344 * 2, 0x00020000);

  unsigned lane16 = (unsigned)lane * 16u;
  unsigned wl = lb + lane16;
  unsigned kofs = (unsigned)kh * 14336u + lane16;
  int rx = r ^ (kh * 16);
  auto relane = [&]() {
    asm volatile("" : "+v"(tid), "+s"(w), "+s"(b));
    lane = tid & 63; r = lane & 31; hh = lane >= 32;
    st = w & 3; kh = w >> 2;
    t = (b & 7) * 2 + (b >> 7); j = (b >> 3) & 15; g = t * 4 + st;
    lane16 = (unsigned)lane * 16u; wl = lb + lane16; kofs = (unsigned)kh * 14336u + lane16;
    rx = r ^ (kh * 16);
  };

  u32x4 xf[14];
  u32x4 hc[12];
  u32x2 hx;
  u32x4 hw[12];
  u32x4 he[2];
  int stn_ = 0; (void)stn_;

  auto fill_c = [&](auto fc, unsigned slot) {
    constexpr int f = decltype(fc)::v;
    const unsigned a = wl + L_R0 + slot * SLOT + (unsigned)w * 3072u;
#pragma unroll
    for (int i = 0; i < 3; ++i) sts128(a + i * 1024, hc[3 * f + i]);
    if (w == f) *(LAS u32x2*)(lb + L_R0 + slot * SLOT + 24576u + (unsigned)lane * 8u) = hx;
  };
  auto fill_w = [&](auto fc, unsigned slot) {
    constexpr int f = decltype(fc)::v;
    const unsigned a = wl + L_R0 + slot * SLOT;
#pragma unroll
    for (int i = 0; i < 3; ++i) sts128(a + (unsigned)w * 3072u + i * 1024, hw[3 * f + i]);
    if (kh == (f & 1)) sts128(a + 24576u + (unsigned)st * 1024u, he[f >> 1]);
  };
  auto store_unit = [&](unsigned base, int kk, u32x4 v) {
    const int u0 = 7 * (j >> 1) + 3 * (j & 1);
    if (kk == 0) {
      const int u = u0 + (hh ? 1 : 0);
      __builtin_amdgcn_raw_buffer_store_b128(v, rs_ws, base + (unsigned)((u >> 1) * 1024 + (u & 1) * 512 + r * 16), 0, 16);
    } else if (!hh) {
      const int u = u0 + 2;
      __builtin_amdgcn_raw_buffer_store_b128(v, rs_ws, base + (unsigned)((u >> 1) * 1024 + (u & 1) * 512 + r * 16), 0, 16);
    } else {
      const int u = 7 * (j >> 1) + 6;
      u32x2 h2; h2[0] = v[0]; h2[1] = v[1];
      __builtin_amdgcn_raw_buffer_store_b64(h2, rs_ws, base + (unsigned)((u >> 1) * 1024 + (u & 1) * 512 + r * 16 + (j & 1) * 8), 0, 16);
    }
  };
  auto load_x14 = [&](unsigned base) {
#pragma unroll
    for (int s = 0; s < 14; ++s) xf[s] = __builtin_amdgcn_raw_buffer_load_b128(rs_ws, base + kofs, (unsigned)(s * 1024), 16);
    SCHED();
  };
  auto set_flag = [&](unsigned fidx) {
    if (tid == 0) __hip_atomic_store((GAS u64*)(wsg + WS_FLAGS) + fidx, FV, __ATOMIC_RELAXED, __HIP_MEMORY_SCOPE_AGENT);
  };
  auto publish = [&](unsigned fidx) { DRAIN(); BAR(); set_flag(fidx); };
  auto await = [&](unsigned fidx) {
    if (w == 0) wait_flags((const GAS u64*)(wsg + WS_FLAGS) + fidx, FV, lane);
    BAR();
  };
  auto cs_add = [&](int idx, float v) {
    if (!hh) __hip_atomic_fetch_add((LAS float*)(lb + L_CS + (unsigned)(idx * 128 + r * 4)), v, __ATOMIC_RELAXED, __HIP_MEMORY_SCOPE_WORKGROUP);
  };
  auto cs_frag = [&](int idx, int row, unsigned biasbits) {
    const float cs = *(LAS float*)(lb + L_CS + (unsigned)(idx * 128 + row * 4));
    const float hi = rb(cs), lo = rb(cs - hi), l2 = cs - hi - lo;
    u32x4 f;
    f[0] = hh ? 0u : pk2(hi, lo);
    f[1] = hh ? 0u : ((pk2(l2, 0.f) & 0xffffu) | biasbits);
    f[2] = 0u; f[3] = 0u;
    return f;
  };
  auto xsend = [&](const f32x16& a) {
    const unsigned ad = wl + L_X + (unsigned)w * 2048u;
    f32x4 x, y;
#pragma unroll
    for (int i = 0; i < 4; ++i) { x[i] = a[8 + i]; y[i] = a[12 + i]; }
    stsf4(ad, x); stsf4(ad + 1024u, y);
  };
  auto xrecv = [&]() {
    const unsigned ad = wl + L_X + (unsigned)(w ^ 4) * 2048u;
    f32x4 x = ldsf4(ad), y = ldsf4(ad + 1024u);
    f32x8 o;
#pragma unroll
    for (int i = 0; i < 4; ++i) { o[i] = x[i]; o[4 + i] = y[i]; }
    return o;
  };
  auto stat_send = [&](float ssq, float sum) {
    f32x2 v; v[0] = ssq; v[1] = sum;
    if (!hh) *(LAS f32x2*)(lb + L_ST + (unsigned)w * 256u + (unsigned)r * 8u) = v;
  };
  auto stat_recv = [&](float ssq, float sum, float& rs, float& sb, u32x4& stf) {
    const f32x2 v = *(LAS f32x2*)(lb + L_ST + (unsigned)(w ^ 4) * 256u + (unsigned)r * 8u);
    ssq += v[0]; sum += v[1];
    const float mean = sum * (1.0f / 448.0f);
    float var = ssq * (1.0f / 448.0f) - mean * mean;
    var = fmaxf(var, 0.f);
    sb = rb(__builtin_sqrtf(rb(rb(var) + 1e-5f)));
    rs = 1.0f / sb;
    stf[0] = hh ? 0u : pk2(-mean, -mean);
    stf[1] = hh ? 0u : pk2(-mean, sb);
    stf[2] = 0u; stf[3] = 0u;
  };
  auto dma1 = [&](decltype(rs_ws) rsrc, unsigned voff, unsigned soff, unsigned dst) {
    unsigned keep;
    asm volatile("s_mov_b32 %0, m0\n\ts_mov_b32 m0, %1\n\ts_nop 0\n\t"
                 "buffer_load_dwordx4 %2, %3, %4 offen lds\n\t"
                 "s_mov_b32 m0, %0"
                 : "=&s"(keep) : "s"(dst), "v"(voff), "s"(rsrc), "s"(soff) : "memory");
  };
  auto bf_load = [&](int img, int fi, unsigned dst) {
    const int tile = fi / 28, s = fi - 28 * tile;
    const int q = lane >> 2, hq = q >> 3;
    int row;
    if (img == 2) row = 16 * fi + 4 * hq + 8 * ((q >> 2) & 1) + (q & 3);
    else { const int u = 2 * s + hq; row = ((q >> 2) & 1 ? unit_f1(u) : unit_f0(u)) + (q & 3); }
    const unsigned ch = (unsigned)(lane & 3) * 16u;
    const unsigned jo = (unsigned)j * 56u - (j == 15 ? 8u : 0u);
    if (img == 0) {
      if (tile < 3) dma1(rs_wq, (unsigned)row * 2688u + ch, jo + (tile == 0 ? 896u : (tile == 1 ? 1792u : 0u)), dst);
      else dma1(rs_wo, (unsigned)row * 896u + ch, jo, dst);
    }
    else if (img == 1) dma1(rs_w1, (unsigned)row * 3584u + ch, (unsigned)j * 224u + (unsigned)((tile + 3) & 3) * 64u - (tile == 0 ? 32u : 0u), dst);
    else dma1(rs_w2, (unsigned)row * 896u + ch, jo, dst);
  };
  auto bf_store = [&](int img, int fi, unsigned src) {
    const int tile = fi / 28;
    int vw = 28, oc = -1, rows = 28; unsigned dst, sh = j == 15 ? 8u : 0u;
    if (img == 0) { oc = tile == 0 ? 28 : -1; rows = tile < 3 ? 29 : 28; dst = tile < 3 ? (unsigned)(fi * 928) : G_WO + (unsigned)((fi - 84) * 896); }
    else if (img == 1) { vw = tile == 0 ? 16 : 32; oc = tile == 0 ? 16 : -1; rows = 32; dst = G_W1 + (unsigned)(fi * 1024); sh = tile == 0 ? 32u : 0u; }
    else dst = G_WO + T28 + (unsigned)(fi * 896);
    const unsigned a = src + sh + (unsigned)((hh ? 512 : 0) + ((lane >> 2) & 3) * 64 + ((lane >> 4) & 1) * 32 + (lane & 3) * 8);
    const u32x2 x0 = trr(a), x1 = trr(a + 256u);
    u32x4 o;
    const unsigned padv = (r == oc) ? 0x3F803F80u : 0u;
    o[0] = r < vw ? x0[0] : padv; o[1] = r < vw ? x0[1] : padv; o[2] = r < vw ? x1[0] : padv; o[3] = r < vw ? x1[1] : padv;
    const bool wr = r < rows || (hh && img == 0 && fi == 83);
    if (wr) __builtin_amdgcn_raw_buffer_store_b128(o, rs_ws, WS_WT + (unsigned)j * G_STRIDE + dst + (unsigned)(((hh ? rows : 0) + r) * 16), 0, 16);
  };

  ST();
  const unsigned bst = lb + L_XS + (unsigned)w * 7680u;
  if (w < 7) { bf_load(0, 7 * t + w, bst); bf_load(1, 7 * t + w, bst + 1024u); bf_load(2, 7 * t + w, bst + 2048u); }
  else {
    auto rs_ph = __builtin_amdgcn_make_buffer_rsrc((void*)P.wphi, 0, 16 * 784 * 2, 0x00020000);
    auto rs_b1 = __builtin_amdgcn_make_buffer_rsrc((void*)P.b1, 0, 1792 * 2, 0x00020000);
    auto rs_b2 = __builtin_amdgcn_make_buffer_rsrc((void*)P.b2, 0, 448 * 2, 0x00020000);
    dma1(rs_ph, lane16, (unsigned)j * 1568u, lb + L_X);
    dma1(rs_ph, lane < 34 ? lane16 + 1024u : 0u, (unsigned)j * 1568u, lb + L_X + 1024u);
    dma1(rs_b1, lane < 14 ? lane16 : 0u, (unsigned)j * 224u, lb + L_X + 2048u);
    dma1(rs_b2, lane < 4 ? lane16 : 0u, (unsigned)j * 56u - (j == 15 ? 8u : 0u), lb + L_X + 3072u);
  }
  u32x4 xr;
  {
    u32x4 raw[14];
    unsigned la[7];
    {
      int row = lane / 14, col = lane - row * 14;
#pragma unroll
      for (int i = 0; i < 7; ++i) {
        const unsigned go = (unsigned)((g * 32 + row) * 896 + kh * 448 + col * 16);
        raw[i] = __builtin_amdgcn_raw_buffer_load_b128(rs_x, go, 0, 0);
        raw[7 + i] = __builtin_amdgcn_raw_buffer_load_b128(rs_x, go, 224, 0);
        la[i] = (unsigned)(row * 240 + col * 16);
        col += 8; row += 4;
        if (col >= 14) { col -= 14; row += 1; }
      }
    }
    {
      const unsigned rowb = (unsigned)(((g * 32 + r) * 448 + 28 * j + (hh ? 4 : 0) + 16 * kh) * 2);
      const u32x2 a = __builtin_amdgcn_raw_buffer_load_b64(rs_x, rowb, 0, 0);
      const u32x2 c = __builtin_amdgcn_raw_buffer_load_b64(rs_x, rowb, 16, 0);
      xr[0] = a[0]; xr[1] = a[1]; xr[2] = c[0]; xr[3] = c[1];
    }
    SCHED();
    ST();
    asm volatile("s_waitcnt vmcnt(16)" ::: "memory");
    if (w < 7) { bf_store(0, 7 * t + w, bst); bf_store(1, 7 * t + w, bst + 1024u); bf_store(2, 7 * t + w, bst + 2048u); }
    else {
      u32x4 z; z[0] = z[1] = z[2] = z[3] = 0u;
      __builtin_amdgcn_raw_buffer_store_b128(z, rs_ws, WS_KV + (unsigned)(((hh ? 16 : 0) + j) * 8192 + t * 512 + r * 16), 0, 16);
    }
    SCHED();
    ST();
    const unsigned ra = bst + (unsigned)r * 240u;
#pragma unroll
    for (int h = 0; h < 2; ++h) {
#pragma unroll
      for (int i = 0; i < 7; ++i) sts128(bst + la[i], raw[7 * h + i]);
      sfor<7>([&](auto ic) {
        constexpr int s = decltype(ic)::v;
        const u32x2 a = *(LAS u32x2*)(ra + (unsigned)(hh ? 2 * unit_f0(2 * s + 1) : 2 * unit_f0(2 * s)));
        const u32x2 c = *(LAS u32x2*)(ra + (unsigned)(hh ? 2 * unit_f1(2 * s + 1) : 2 * unit_f1(2 * s)));
        xf[7 * h + s][0] = a[0]; xf[7 * h + s][1] = a[1]; xf[7 * h + s][2] = c[0]; xf[7 * h + s][3] = c[1];
      });
    }
    SCHED();
    ST();
    E = (u64)__builtin_amdgcn_readfirstlane((unsigned)E) | ((u64)__builtin_amdgcn_readfirstlane((unsigned)(E >> 32)) << 32);
    FV = (E + 1ull) * 0x9E3779B97F4A7C15ull + 0x632BE59BD9B4E019ull;
  }
  publish(10u * 256u + (unsigned)(j * 16 + t));
  ST();
  if (w == 7) {
    const unsigned pa = lb + L_X + (unsigned)(r < 28 ? r : 0) * 2u;
    unsigned tv[16];
#pragma unroll
    for (int e = 0; e < 16; ++e) {
      const int d0 = 16 * (e >> 3) + 8 * ((e >> 2) & 1) + (e & 3);
      const int d = d0 + (hh ? 4 : 0);
      tv[e] = *(LAS unsigned short*)(pa + (unsigned)((d < 28 ? d : 0) * 56));
    }
#pragma unroll
    for (int i = 0; i < 2; ++i) {
      u32x4 o;
#pragma unroll
      for (int e = 0; e < 4; ++e) {
        const bool ok = r < 28 && !(hh && i == 1 && e >= 2);
        o[e] = ok ? (tv[8 * i + 2 * e] | (tv[8 * i + 2 * e + 1] << 16)) : 0u;
      }
      sts128(wl + L_WPHI + (unsigned)i * 1024u, o);
    }
  } else if (w == 1) {
#pragma unroll
    for (int i = 0; i < 2; ++i) {
      u32x4 v;
#pragma unroll
      for (int e = 0; e < 8; e += 2) {
        const int d0 = 16 * i + 8 * (e >> 2) + (hh ? 4 : 0) + (e & 3);
        v[e >> 1] = (d0 == r ? 0x3F80u : 0u) | (d0 + 1 == r ? 0x3F800000u : 0u);
      }
      sts128(wl + L_ID + (unsigned)i * 1024u, v);
    }
  } else if (w == 2) {
    const unsigned v0 = *(LAS unsigned short*)(lb + L_X + 2048u + (unsigned)lane * 2u);
    const unsigned v1 = *(LAS unsigned short*)(lb + L_X + 2048u + 128u + (unsigned)lane * 2u);
    const unsigned v2 = *(LAS unsigned short*)(lb + L_X + 3072u + (j == 15 ? 8u : 0u) + (unsigned)(r < 28 ? r : 0) * 2u);
    *(LAS unsigned*)(lb + L_B1 + (unsigned)lane * 4u) = v0 << 16;
    *(LAS unsigned*)(lb + L_B1 + 256u + (unsigned)lane * 4u) = lane < 48 ? v1 << 16 : 0u;
    if (lane < 32) *(LAS float*)(lb + L_B2 + (unsigned)lane * 4u) = r < 28 ? bflo(v2) : 0.f;
  } else if (w == 4) {
    f32x4 z; z[0] = z[1] = z[2] = z[3] = 0.f;
    if (lane < 56) stsf4(lb + L_CS + lane16, z);
  }
  await(10u * 256u + (unsigned)j * 16u);
  ST();
  {
    const int f0 = w < 7 ? 10 * w : 67;
    const unsigned so = WS_WT + (unsigned)j * G_STRIDE + (unsigned)f0 * 1024u;
    const unsigned ld = lb + L_K + (unsigned)f0 * 1024u;
    unsigned keep;
    asm volatile("s_mov_b32 %0, m0\n\ts_mov_b32 m0, %1\n\ts_nop 0\n\t"
                 "buffer_load_dwordx4 %4, %5, %6 offen sc1 lds\n\t"
                 "buffer_load_dwordx4 %4, %5, %6 offen offset:1024 sc1 lds\n\t"
                 "buffer_load_dwordx4 %4, %5, %6 offen offset:2048 sc1 lds\n\t"
                 "buffer_load_dwordx4 %4, %5, %6 offen offset:3072 sc1 lds\n\t"
                 "s_mov_b32 m0, %2\n\ts_nop 0\n\t"
                 "buffer_load_dwordx4 %4, %5, %7 offen sc1 lds\n\t"
                 "buffer_load_dwordx4 %4, %5, %7 offen offset:1024 sc1 lds\n\t"
                 "buffer_load_dwordx4 %4, %5, %7 offen offset:2048 sc1 lds\n\t"
                 "buffer_load_dwordx4 %4, %5, %7 offen offset:3072 sc1 lds\n\t"
                 "s_mov_b32 m0, %3\n\ts_nop 0\n\t"
                 "buffer_load_dwordx4 %4, %5, %8 offen sc1 lds\n\t"
                 "buffer_load_dwordx4 %4, %5, %8 offen offset:1024 sc1 lds\n\t"
                 "s_mov_b32 m0, %0"
                 : "=&s"(keep) : "s"(ld), "s"(ld + 4096u), "s"(ld + 8192u), "v"(lane16), "s"(rs_ws), "s"(so), "s"(so + 4096u), "s"(so + 8192u) : "memory");
    DRAIN();
    BAR();
  }
  ST();
  {
    u32x4 on; on[0] = on[1] = on[2] = on[3] = 0x3F803F80u;
    const unsigned a = lb + L_K + (unsigned)(((hh ? 29 : 0) + r) * 16 + w * 928);
    u32x4 cf[12];
#pragma unroll
    for (int c = 0; c < 3; ++c)
#pragma unroll
      for (int i = 0; i < 4; ++i) cf[4 * c + i] = lds128(a + c * T29 + (i < 3 ? i * 8 * 928 : (w < 4 ? 24 * 928 : 0)));
    u32x4 o3; o3[0] = o3[1] = o3[2] = o3[3] = (w < 4 ? 0x3F803F80u : 0u);
    f32x16 s0 = MFMA(on, cf[0], zero16()), s1 = MFMA(on, cf[4], zero16()), s2 = MFMA(on, cf[8], zero16());
#pragma unroll
    for (int i = 1; i < 4; ++i) {
      s0 = MFMA(i < 3 ? on : o3, cf[i], s0); s1 = MFMA(i < 3 ? on : o3, cf[4 + i], s1); s2 = MFMA(i < 3 ? on : o3, cf[8 + i], s2);
    }
    cs_add(0, s0[0]); cs_add(1, s1[0]); cs_add(2, s2[0]);
  }
#pragma unroll
  for (int i = 0; i < 12; ++i) { hc[i][0] = 0u; hc[i][1] = 0u; hc[i][2] = 0u; hc[i][3] = 0u; hw[i] = hc[i]; }
  hx[0] = 0u; hx[1] = 0u; he[0] = hc[0]; he[1] = hc[0];

  for (int layer = 0; layer < 2; ++layer) {
    const unsigned fb = (unsigned)layer * 5u * 256u;
    relane();
    if (layer == 1) {
      await(4u * 256u + (unsigned)t * 16u);
      load_x14(WS_X2 + (unsigned)g * 28672u);
    }
    ST();
    float rs, sb; u32x4 stf;
    {
      const unsigned wq = lb + L_K + (unsigned)(((hh ? 29 : 0) + r) * 16 + kh * (14 * 928));
      float sq0 = 0.f, sq1 = 0.f;
      f32x16 a1 = gemm_f<14, 928>(wq + (unsigned)(1 - kh) * T29, xf, zero16(), [&](auto ic) { ssq_step(xf[decltype(ic)::v], sq0, sq1); });
      st16(wl + L_R0 + (unsigned)w * 4096u, a1);
      f32x16 am = gemm<14, 928>(wq + (unsigned)kh * T29, xf, zero16());
      const float ssq = xadd(sq0 + sq1), sum = xadd(hh ? (kh ? a1[12] : am[12]) : 0.f);
      stat_send(ssq, sum);
      ST();
      BAR();
      if (layer == 0) {
        const unsigned ib = WS_WT + (unsigned)j * G_STRIDE;
        const unsigned vo = (unsigned)w * 3072u + lane16;
#pragma unroll
        for (int f = 0; f < 4; ++f)
#pragma unroll
          for (int i = 0; i < 3; ++i) hw[3 * f + i] = __builtin_amdgcn_raw_buffer_load_b128(rs_ws, vo + i * 1024, ib + G_W1 + f * SLOT, 16);
#pragma unroll
        for (int m = 0; m < 2; ++m)
          he[m] = __builtin_amdgcn_raw_buffer_load_b128(rs_ws, lane16, ib + G_W1 + (unsigned)(2 * m + kh) * SLOT + (unsigned)(24 + st) * 1024u, 16);
        SCHED();
      }
      stat_recv(ssq, sum, rs, sb, stf);
      am = MFMA(cs_frag(kh, r, 0u), stf, am);
      add16(wl + L_R0 + (unsigned)(w ^ 4) * 4096u, am);
      f32x16 aq;
      if (kh == 0) {
        u32x4 mf0, mf1;
        pack16(am, mf0, mf1);
        const u32x4 wp0 = lds128(wl + L_WPHI), wp1 = lds128(wl + L_WPHI + 1024u);
        f32x16 pk = MFMA(wp0, mf0, zero16());
        pk = MFMA(wp1, mf1, pk);
        u32x4 kp0, kp1, km0, km1;
        dual_softmax(pk, hh, rs, rs, kp0, kp1, km0, km1);
        const u32x4 id0 = lds128(wl + L_ID), id1 = lds128(wl + L_ID + 1024u);
        f32x16 tp = MFMA(kp0, id0, zero16()); tp = MFMA(kp1, id1, tp);
        f32x16 tm = MFMA(km0, id0, zero16()); tm = MFMA(km1, id1, tm);
#pragma unroll
        for (int i = 0; i < 8; ++i) {
          aq[i] = __builtin_bit_cast(float, pk2(tp[2 * i], tp[2 * i + 1]));
          aq[8 + i] = __builtin_bit_cast(float, pk2(tm[2 * i], tm[2 * i + 1]));
        }
      } else {
        am[12] = hh ? sb : am[12];
#pragma unroll
        for (int i = 13; i < 16; ++i) am[i] = hh ? 0.f : am[i];
        u32x4 mf0, mf1;
        pack16(am, mf0, mf1);
        const u32x4 id0 = lds128(wl + L_ID), id1 = lds128(wl + L_ID + 1024u);
        f32x16 tv = MFMA(mf0, id0, zero16()); tv = MFMA(mf1, id1, tv);
        u32x4 tv0, tv1;
        pack16(tv, tv0, tv1);
        sts128(wl + L_X + (unsigned)st * 2048u, tv0); sts128(wl + L_X + (unsigned)st * 2048u + 1024u, tv1);
        aq = MFMA(cs_frag(2, r, 0u), stf, zero16());
        aq = gemm<14, 928>(wq + 2u * T29, xf, aq);
      }
      ST();
      BAR();
      if (kh == 0) {
        const u32x4 tv0 = lds128(wl + L_X + (unsigned)st * 2048u), tv1 = lds128(wl + L_X + (unsigned)st * 2048u + 1024u);
        u32x4 tp0, tp1, tm0, tm1;
#pragma unroll
        for (int i = 0; i < 4; ++i) {
          tp0[i] = f2u(aq[i]); tp1[i] = f2u(aq[4 + i]); tm0[i] = f2u(aq[8 + i]); tm1[i] = f2u(aq[12 + i]);
        }
        f32x16 kvp = MFMA(tp0, tv0, zero16()); kvp = MFMA(tp1, tv1, kvp);
        f32x16 kvm = MFMA(tm0, tv0, zero16()); kvm = MFMA(tm1, tv1, kvm);
        st16(wl + L_R0 + (unsigned)st * 8192u, kvp);
        st16(wl + L_R0 + (unsigned)st * 8192u + 4096u, kvm);
      }
      BAR();
      {
        const unsigned a = lb + L_R0 + (unsigned)w * 1024u + (unsigned)lane * 4u;
        const unsigned gb = WS_KV + (unsigned)(layer * 16 + j) * 8192u + (unsigned)w * 1024u;
#pragma unroll
        for (int i = 0; i < 4; ++i) {
          float s = *(LAS float*)(a + i * 256);
#pragma unroll
          for (int ww = 1; ww < 4; ++ww) s += *(LAS float*)(a + i * 256 + ww * 8192);
          __builtin_amdgcn_raw_ptr_buffer_atomic_fadd_f32(s, rs_ws, (unsigned)lane * 4u + i * 256, gb, 0);
        }
      }
      ST();
      if (kh == 0) {
        aq = gemm<14, 928>(wq + 2u * T29, xf, zero16());
        st16(wl + L_X + (unsigned)st * 4096u, aq);
      }
      BAR();
      if (kh == 1) {
        add16(wl + L_X + (unsigned)st * 4096u, aq);
        u32x4 qf0, qf1;
        pack16(aq, qf0, qf1);
        const u32x4 wp0 = lds128(wl + L_WPHI), wp1 = lds128(wl + L_WPHI + 1024u);
        f32x16 pq = MFMA(wp0, qf0, zero16());
        pq = MFMA(wp1, qf1, pq);
        dual_softmax(pq, hh, rs, 1.0f, xf[0], xf[1], xf[2], xf[3]);
      }
      ST();
      publish(fb + 0u * 256u + (unsigned)(j * 16 + t));
      ST();
      {
        const unsigned so = WS_WT + (unsigned)j * G_STRIDE + G_WO + (unsigned)w * 3072u;
        const unsigned ld = lb + L_R0 + (unsigned)w * 3072u;
        unsigned keep;
        asm volatile("s_mov_b32 %0, m0\n\ts_mov_b32 m0, %1\n\ts_nop 0\n\t"
                     "buffer_load_dwordx4 %2, %3, %4 offen sc1 lds\n\t"
                     "buffer_load_dwordx4 %2, %3, %4 offen offset:1024 sc1 lds\n\t"
                     "buffer_load_dwordx4 %2, %3, %4 offen offset:2048 sc1 lds\n\t"
                     "buffer_load_dwordx4 %2, %3, %4 offen offset:3072 sc1 lds\n\t"
                     "s_mov_b32 m0, %0"
                     : "=&s"(keep) : "s"(ld), "v"(lane16), "s"(rs_ws), "s"(so) : "memory");
      }
      fill_w(IC<0>{}, 1u);
      if (layer == 0) {
        const unsigned ib = WS_WT + (unsigned)j * G_STRIDE + G_WO + T28;
        const unsigned vo = (unsigned)w * 3072u + lane16;
#pragma unroll
        for (int f = 0; f < 4; ++f)
#pragma unroll
          for (int i = 0; i < 3; ++i) hc[3 * f + i] = __builtin_amdgcn_raw_buffer_load_b128(rs_ws, vo + i * 1024, ib + f * T28, 16);
        hx = __builtin_amdgcn_raw_buffer_load_b64(rs_ws, (unsigned)lane * 8u, ib + (unsigned)st * T28 + 24576u, 16);
        u32x4 on; on[0] = on[1] = on[2] = on[3] = 0x3F803F80u;
#pragma unroll
        for (int f = 0; f < 4; ++f) {
          f32x16 s = MFMA(on, hw[3 * f], zero16());
          s = MFMA(on, hw[3 * f + 1], s);
          s = MFMA(on, hw[3 * f + 2], s);
          if (kh == (f & 1)) s = MFMA(on, he[f >> 1], s);
          cs_add(3 + f, s[0]);
        }
      }
    }
    relane();
    ST();
    await(fb + 0u * 256u + (unsigned)j * 16u);
    ST();
    if (layer == 1 && b == 0 && tid == 0)
      __hip_atomic_store((GAS u64*)(wsg + WS_EPOCH), E + 1ull, __ATOMIC_RELAXED, __HIP_MEMORY_SCOPE_AGENT);
    {
      const f32x4 sa = __builtin_bit_cast(f32x4, __builtin_amdgcn_raw_buffer_load_b128(rs_ws, lane16,
          WS_KV + (unsigned)(layer * 16 + j) * 8192u + (unsigned)w * 1024u, 16));
      u32x2 v; v[0] = pk2(sa[0], sa[1]); v[1] = pk2(sa[2], sa[3]);
      *(LAS u32x2*)(wl + L_X + (unsigned)(w >> 1) * 1024u + (unsigned)(w & 1) * 8u) = v;
      BAR();
      if (kh == 1) {
        const u32x4 k0 = lds128(wl + L_X), k1 = lds128(wl + L_X + 1024u);
        const u32x4 k2 = lds128(wl + L_X + 2048u), k3 = lds128(wl + L_X + 3072u);
        f32x16 nu = MFMA(k0, xf[0], zero16());
        nu = MFMA(k1, xf[1], nu); nu = MFMA(k2, xf[2], nu); nu = MFMA(k3, xf[3], nu);
        float den = xadd(hh ? nu[12] : 0.f);
        den = rb(rb(den) + 1e-6f);
        const float rd = 1.0f / den;
#pragma unroll
        for (int i = 0; i < 16; ++i) nu[i] = rb(nu[i]) * rd;
        u32x4 o0, o1;
        pack16(nu, o0, o1);
        const unsigned ob = WS_O + (unsigned)(layer * 64 + g) * 28672u;
        store_unit(ob, 0, o0); store_unit(ob, 1, o1);
      }
    }
    ST();
    publish(fb + 1u * 256u + (unsigned)(t * 16 + j));
    relane();
    ST();
    await(fb + 1u * 256u + (unsigned)t * 16u);
    ST();
    load_x14(WS_O + (unsigned)(layer * 64 + g) * 28672u);
    {
      const unsigned wc = lb + L_R0 + (unsigned)(((hh ? 28 : 0) + rx) * 16 + kh * (14 * 896));
      f32x16 a = gemm<14, 896>(wc, xf, zero16());
      xsend(a);
      BAR();
      fill_w(IC<1>{}, 0u);
      const f32x8 o = xrecv();
#pragma unroll
      for (int q = 0; q < 4; ++q)
        xr[q] = pk2(bflo(xr[q]) + rb(a[2 * q] + o[2 * q]), bfhi(xr[q]) + rb(a[2 * q + 1] + o[2 * q + 1]));
      store_unit(WS_X1 + (unsigned)(layer * 64 + g) * 28672u, kh, xr);
    }
    ST();
    publish(fb + 2u * 256u + (unsigned)(t * 16 + j));
    relane();
    ST();
    await(fb + 2u * 256u + (unsigned)t * 16u);
    ST();
    load_x14(WS_X1 + (unsigned)(layer * 64 + g) * 28672u);
    {
      const unsigned hb = WS_H + (unsigned)(layer * 64 + g) * 114688u + (unsigned)(7 * j + kh) * 1024u + lane16;
      const unsigned ww = lb + L_R0 + (unsigned)(((hh ? 32 : 0) + rx) * 16 + kh * 14336);
      auto bias = [&](int c) { return *(LAS unsigned*)(lb + L_B1 + (unsigned)(c * 128 + rx * 4)); };
      float sq0 = 0.f, sq1 = 0.f;
      f32x16 a0 = gemm_f<14, 1024>(ww + SLOT, xf, zero16(), [&](auto ic) { ssq_step(xf[decltype(ic)::v], sq0, sq1); });
      const float ssq = xadd(sq0 + sq1), sum = xadd(hh ? 0.f : (kh ? a0[0] : a0[8]));
      stat_send(ssq, sum);
      xsend(a0);
      ST();
      BAR();
      ST();
      fill_w(IC<2>{}, 1u);
      stat_recv(ssq, sum, rs, sb, stf);
      a0 = MFMA(cs_frag(3, rx, bias(3)), stf, a0);
      f32x8 ap, uu = xrecv();
#pragma unroll
      for (int i = 0; i < 8; ++i) ap[i] = a0[i];
      const float c1 = (-1.5957691216057308f * 1.4426950408889634f) * rs;
      const float c2 = c1 * 0.044715f * rs * rs;
      auto epi_item = [&](auto stc, auto elc) {
        constexpr int sg = decltype(stc)::v, i = decltype(elc)::v;
        if constexpr (sg == 0) ap[i] += uu[i];
        else if constexpr (sg == 1) uu[i] = ap[i] * ap[i];
        else if constexpr (sg == 2) uu[i] = uu[i] * c2 + c1;
        else if constexpr (sg == 3) uu[i] = uu[i] * ap[i];
        else if constexpr (sg == 4) uu[i] = __builtin_amdgcn_exp2f(uu[i]);
        else if constexpr (sg == 5) uu[i] = uu[i] * sb + sb;
        else if constexpr (sg == 6) uu[i] = __builtin_amdgcn_rcpf(uu[i]);
        else ap[i] = ap[i] * uu[i];
      };
      auto epi_store = [&](int c) {
        u32x4 f0;
#pragma unroll
        for (int i = 0; i < 4; ++i) f0[i] = pk2(ap[2 * i], ap[2 * i + 1]);
        if (c != 3 || kh == 0) __builtin_amdgcn_raw_buffer_store_b128(f0, rs_ws, hb + (unsigned)(2 * c) * 1024u, 0, 16);
      };
      sfor<3>([&](auto ccc) {
        constexpr int cc = decltype(ccc)::v + 1;
        f32x16 an = zero16();
        if (kh == (cc & 1)) an = MFMA(cs_frag(3 + cc, rx, bias(cc - 1)), stf, an);
        an = gemm_f<14, 1024>(ww + (unsigned)((cc + 1) & 1) * SLOT, xf, an, [&](auto ic) {
          constexpr int s = decltype(ic)::v;
          if constexpr (s < 13) { sfor<5>([&](auto kc) { constexpr int it = 5 * s + decltype(kc)::v; if constexpr (it < 64) epi_item(IC<it / 8>{}, IC<it % 8>{}); }); }
          else epi_store((cc + 2) & 3);
          if constexpr (s == 6) BAR0();
        });
        xsend(an);
        ST();
        BAR();
        ST();
        if constexpr (cc == 1) fill_w(IC<3>{}, 0u);
        else if constexpr (cc == 2) fill_c(IC<0>{}, 1u);
        else fill_c(IC<1>{}, 0u);
        uu = xrecv();
#pragma unroll
        for (int i = 0; i < 8; ++i) ap[i] = an[i];
      });
      sfor<64>([&](auto kc) { constexpr int it = decltype(kc)::v; epi_item(IC<it / 8>{}, IC<it % 8>{}); });
      epi_store(2);
    }
    ST();
    publish(fb + 3u * 256u + (unsigned)(t * 16 + j));
    relane();
    ST();
    await(fb + 3u * 256u + (unsigned)t * 16u);
    ST();
    {
      unsigned hb = WS_H + (unsigned)(layer * 64 + g) * 114688u + kofs;
      asm volatile("" : "+v"(hb));
      auto ldx = [&](auto ic) { constexpr int s = decltype(ic)::v;
        xf[s % 14] = __builtin_amdgcn_raw_buffer_load_b128(rs_ws, hb, (unsigned)((s / 14) * 28672 + (s % 14) * 1024), 16); };
      sfor<14>(ldx);
      SCHED();
      f32x16 a = zero16();
      {
        unsigned wb = lb + L_R0 + (unsigned)(((hh ? 28 : 0) + rx) * 16 + kh * (14 * 896));
        asm volatile("" : "+v"(wb));
        u32x4 wf[PD + 1];
        auto ldw = [&](auto ic) { constexpr int s = decltype(ic)::v;
          wf[s % (PD + 1)] = lds128(wb + (unsigned)((((s / 14) + 1) & 1) * SLOT + (s % 14) * 896)); };
        sfor<PD>(ldw);
        SCHED();
        sfor<56>([&](auto ic) {
          constexpr int s = decltype(ic)::v;
          if constexpr ((s + PD) % 14 == 0 && s + PD < 56) {
            BAR();
            if constexpr (s + PD == 14) fill_c(IC<2>{}, 1u);
            if constexpr (s + PD == 28) fill_c(IC<3>{}, 0u);
          }
          if constexpr (s + PD < 56) ldw(IC<s + PD>{});
          a = MFMA(wf[s % (PD + 1)], xf[s % 14], a);
          if constexpr (s + 14 < 56) ldx(IC<s + 14>{});
          SCHED();
        });
      }
      xsend(a);
      BAR();
      const f32x8 o = xrecv();
      f32x8 bb;
#pragma unroll
      for (int q = 0; q < 2; ++q) {
        const f32x4 v = ldsf4(lb + L_B2 + (unsigned)(kh * 64 + q * 32) + (hh ? 16u : 0u));
        bb[4 * q] = v[0]; bb[4 * q + 1] = v[1]; bb[4 * q + 2] = v[2]; bb[4 * q + 3] = v[3];
      }
#pragma unroll
      for (int q = 0; q < 4; ++q)
        xr[q] = pk2(bflo(xr[q]) + rb(rb(a[2 * q] + o[2 * q]) + bb[2 * q]), bfhi(xr[q]) + rb(rb(a[2 * q + 1] + o[2 * q + 1]) + bb[2 * q + 1]));
    }
    ST();
    if (layer == 0) {
      store_unit(WS_X2 + (unsigned)g * 28672u, kh, xr);
      publish(4u * 256u + (unsigned)(t * 16 + j));
    } else {
      const unsigned rowb = (unsigned)(((g * 32 + r) * 448 + 28 * j + (hh ? 4 : 0) + 16 * kh) * 2);
      u32x2 a, c; a[0] = xr[0]; a[1] = xr[1]; c[0] = xr[2]; c[1] = xr[3];
      __builtin_amdgcn_raw_buffer_store_b64(a, rs_out, rowb, 0, 0);
      if (kh == 0 || !hh) __builtin_amdgcn_raw_buffer_store_b64(c, rs_out, rowb + 16, 0, 0);
    }
    ST();
  }
}

#if defined(__HIP_DEVICE_COMPILE__)
#pragma clang attribute pop
#endif

extern "C" void kernel_launch(void* const* d_in, const int* in_sizes, int n_in,
                              void* d_out, int out_size, void* d_ws, size_t ws_size,
                              hipStream_t stream) {
  Params p;
  p.x = d_in[0]; p.wqkv = d_in[1]; p.wphi = d_in[2]; p.wo = d_in[3];
  p.w1 = d_in[5]; p.b1 = d_in[6]; p.w2 = d_in[7]; p.b2 = d_in[8];
  p.out = d_out; p.ws = d_ws;
  fwd<<<256, 512, 0, stream>>>(p);
}
```

```cpp
#include <hip/hip_runtime.h>
#include <hip/hip_bf16.h>
#include <stdint.h>
#include <utility>

#if defined(__HIP_DEVICE_COMPILE__)
#pragma clang attribute push(__attribute__((target("no-packed-fp32-ops"))), apply_to = function)
#endif

typedef __attribute__((ext_vector_type(2))) __bf16 bf16x2;
typedef __attribute__((ext_vector_type(8))) __bf16 bf16x8;
typedef __attribute__((ext_vector_type(16))) float f32x16;
typedef __attribute__((ext_vector_type(8))) float f32x8;
typedef __attribute__((ext_vector_type(4))) float f32x4;
typedef __attribute__((ext_vector_type(2))) float f32x2;
typedef __attribute__((ext_vector_type(2))) unsigned u32x2;
typedef __attribute__((ext_vector_type(4))) unsigned u32x4;
typedef unsigned long long u64;
#define LAS __attribute__((address_space(3)))
#define GAS __attribute__((address_space(1)))
#define DI __device__ __forceinline__
#define MFMA(a, b, c) __builtin_amdgcn_mfma_f32_32x32x16_bf16(__builtin_bit_cast(bf16x8, (a)), __builtin_bit_cast(bf16x8, (b)), (c), 0, 0, 0)

#define WS_EPOCH 0u
#define WS_FLAGS 4096u
#define FSK 1
#define FSI 1
#define FST 16
#define FK(l, i) ((GAS u64*)(wsg + WS_FLAGS) + (l) * 4096 + (i) * FSK)
#define FI(i) ((GAS u64*)(wsg + WS_FLAGS) + 8192 + (i) * FSI)
#define FT(i) ((GAS u64*)(wsg + WS_FLAGS) + 12288 + (i) * FST)
#define WS_KV    196608u
#define WS_O     458752u
#define WS_X1    2293760u
#define WS_X2    4128768u
#define WS_H     5963776u
#define WS_WT    13303808u
#define G_WO     78848u
#define G_W1     204288u
#define G_CS     318976u
#define G_STRIDE 344576u
#define WS_END   (WS_WT + 16u * G_STRIDE)
#define SENT     0xFFFFFFFFu

#define L_K     0u
#define L_Q     57360u
#define T29     25984u
#define L_R0    83392u
#define SLOT    28672u
#define T28     25088u
#define L_X     140736u
#define L_XS    83392u
#define L_RAW   78864u
#define L_WPHI  157120u
#define L_ID    159168u
#define L_CP    160192u
#define L_CF    161088u
#define L_B2    162368u
#define L_CNT   162496u
#define L_END   162512u

#define ROT 0
#define FM 1
#define PSL 1
#define PSL2 1
#define NOPRB 0
#define SL0 6
#define PFO 0
#define L2P 3
#define KP 2
#define CT 10
#define SK 1
#define Q4 0
#define HQ 1
#define CH 0
#define AF 3
#define PW 1
#define FR 1
#define MF 1
#define PF 1
#define TD 64u
#define FD 1
#define PM 1
#define SPIN_CAP (1u << 20)

DI unsigned pk2(float a, float b) { bf16x2 v; v[0] = (__bf16)a; v[1] = (__bf16)b; return __builtin_bit_cast(unsigned, v); }
DI unsigned f2u(float a) { return __builtin_bit_cast(unsigned, a); }
DI float rb(float a) { return (float)(__bf16)a; }
DI float bflo(unsigned u) { return __builtin_bit_cast(float, u << 16); }
DI float bfhi(unsigned u) { return __builtin_bit_cast(float, u & 0xffff0000u); }
DI float xadd(float a) { auto r = __builtin_amdgcn_permlane32_swap(__builtin_bit_cast(unsigned, a), __builtin_bit_cast(unsigned, a), false, false);
                         return __builtin_bit_cast(float, (unsigned)r[0]) + __builtin_bit_cast(float, (unsigned)r[1]); }
DI float xmax(float a) { auto r = __builtin_amdgcn_permlane32_swap(__builtin_bit_cast(unsigned, a), __builtin_bit_cast(unsigned, a), false, false);
                         return fmaxf(__builtin_bit_cast(float, (unsigned)r[0]), __builtin_bit_cast(float, (unsigned)r[1])); }
DI float xmin(float a) { auto r = __builtin_amdgcn_permlane32_swap(__builtin_bit_cast(unsigned, a), __builtin_bit_cast(unsigned, a), false, false);
                         return fminf(__builtin_bit_cast(float, (unsigned)r[0]), __builtin_bit_cast(float, (unsigned)r[1])); }

#define ST() do {} while (0)
#define RT(i) do {} while (0)
#define CNT() do {} while (0)
#define CST(i) do {} while (0)
#define ST2() do {} while (0)
static_assert(MF || !FR, "");
#define SCHED() __builtin_amdgcn_sched_barrier(0)
#define BAR() do { SCHED(); asm volatile("s_waitcnt lgkmcnt(0)\n\ts_barrier" ::: "memory"); SCHED(); } while (0)
#define BAR0() do { SCHED(); asm volatile("s_barrier" ::: "memory"); SCHED(); } while (0)
#define DRAIN() asm volatile("s_waitcnt vmcnt(0)" ::: "memory")

typedef __attribute__((ext_vector_type(4))) __bf16 bf16x4;
DI u32x2 trr(unsigned addr) { return __builtin_bit_cast(u32x2, __builtin_amdgcn_ds_read_tr16_b64_v4bf16((LAS bf16x4*)addr)); }
DI u32x4 lds128(unsigned addr) { return *(LAS u32x4*)addr; }
DI void sts128(unsigned addr, u32x4 v) { *(LAS u32x4*)addr = v; }
DI f32x4 ldsf4(unsigned addr) { return *(LAS f32x4*)addr; }
DI void stsf4(unsigned addr, f32x4 v) { *(LAS f32x4*)addr = v; }

DI void ld_dev(u32x4& d, __amdgpu_buffer_rsrc_t rs, unsigned vo, unsigned so) {
  asm volatile("buffer_load_dwordx4 %0, %1, %2, %3 offen sc1" : "=v"(d) : "v"(vo), "s"(rs), "s"(so) : "memory");
}
template <int N> DI void wait_vm(u32x4& x, int on) {
  asm volatile("s_cmp_eq_u32 %2, 0\n\ts_cbranch_scc1 .Lwv%=\n\ts_waitcnt vmcnt(%1)\n.Lwv%=:" : "+v"(x) : "n"(N), "s"(on) : "memory", "scc");
}
DI void wait_u64(u64& x) { asm volatile("s_waitcnt vmcnt(0)" : "+v"(x) :: "memory"); }
DI u32x4 ldt(unsigned a) { const u32x2 x0 = trr(a), x1 = trr(a + 256u); u32x4 o; o[0] = x0[0]; o[1] = x0[1]; o[2] = x1[0]; o[3] = x1[1]; return o; }

DI void st128(u32x4 v, __amdgpu_buffer_rsrc_t rs, unsigned vo, unsigned so, int) {
  __builtin_amdgcn_raw_buffer_store_b128(v, rs, vo, so, 16);
  asm volatile("s_nop 1" :: "v"(v));
}
template <int I> struct IC { static constexpr int v = I; };
template <class F, int... Is> DI void sfor_impl(F&& f, std::integer_sequence<int, Is...>) { (f(IC<Is>{}), ...); }
template <int N, class F> DI void sfor(F&& f) { sfor_impl(f, std::make_integer_sequence<int, N>{}); }

#define PD 3
#define NA 5
#define NB 5
#define NC 4
#define ND 0
#define NE 0
#define NF 0
#define NG 7
#define NH 6
#define S0 2
#define PRI 0
#define HR 3
#define TP 0
#define SL1 2
#define SL3 12
#define SL4 4
template <int NS, int STR, class G, class F>
DI f32x16 gemm_p(unsigned wb, const u32x4* xf, f32x16 acc, G&& pre, F&& f) {
  asm volatile("" : "+v"(wb));
  u32x4 wf[NS];
  SCHED();
#pragma unroll
  for (int s = 0; s < PD; ++s) wf[s] = lds128(wb + s * STR);
  SCHED();
  sfor<NS>([&](auto ic) {
    constexpr int s = decltype(ic)::v;
    if constexpr (s + PD < NS) wf[s + PD] = lds128(wb + (s + PD) * STR);
    pre(ic);
    acc = MFMA(wf[s], xf[s], acc);
    f(ic);
    SCHED();
  });
  return acc;
}
template <int NS, int STR, class F>
DI f32x16 gemm_f(unsigned wb, const u32x4* xf, f32x16 acc, F&& f) { return gemm_p<NS, STR>(wb, xf, acc, [](auto) {}, f); }
template <int NS, int STR>
DI f32x16 gemm(unsigned wb, const u32x4* xf, f32x16 acc) { return gemm_p<NS, STR>(wb, xf, acc, [](auto) {}, [](auto) {}); }

DI f32x16 zero16() { f32x16 z;
#pragma unroll
  for (int i = 0; i < 16; ++i) z[i] = 0.f;
  return z; }

DI void pack16(const f32x16& v, u32x4& f0, u32x4& f1) {
#pragma unroll
  for (int i = 0; i < 4; ++i) { f0[i] = pk2(v[2 * i], v[2 * i + 1]); f1[i] = pk2(v[8 + 2 * i], v[8 + 2 * i + 1]); }
}
DI void st16(unsigned a, const f32x16& v) {
#pragma unroll
  for (int q = 0; q < 4; ++q) { f32x4 x; x[0] = v[4 * q]; x[1] = v[4 * q + 1]; x[2] = v[4 * q + 2]; x[3] = v[4 * q + 3]; stsf4(a + q * 1024, x); }
}
DI void add16(unsigned a, f32x16& v) {
#pragma unroll
  for (int q = 0; q < 4; ++q) { f32x4 x = ldsf4(a + q * 1024); v[4 * q] += x[0]; v[4 * q + 1] += x[1]; v[4 * q + 2] += x[2]; v[4 * q + 3] += x[3]; }
}
template <class F> DI void add16f(unsigned a, f32x16& v, F&& f) {
  f32x4 x[4];
#pragma unroll
  for (int q = 0; q < 4; ++q) x[q] = ldsf4(a + q * 1024);
  SCHED();
  f();
#pragma unroll
  for (int i = 0; i < 16; ++i) v[i] += x[i >> 2][i & 3];
}

DI void softmax28(f32x16 p, bool hh, int neg, float rs, float os, u32x4& p0, u32x4& p1) {
#pragma unroll
  for (int i = 12; i < 16; ++i) p[i] = hh ? p[0] : p[i];
  float mx = p[0];
  if (neg) {
#pragma unroll
    for (int i = 1; i < 16; ++i) mx = fminf(mx, p[i]);
    mx = xmin(mx);
  } else {
#pragma unroll
    for (int i = 1; i < 16; ++i) mx = fmaxf(mx, p[i]);
    mx = xmax(mx);
  }
  const float L = (neg ? -1.4426950408889634f : 1.4426950408889634f) * rs;
  const float mxl = mx * L;
  float sp = 0.f;
#pragma unroll
  for (int i = 0; i < 16; ++i) {
    float a = __builtin_amdgcn_exp2f(p[i] * L - mxl);
    if (i >= 12) a = hh ? 0.f : a;
    p[i] = a; sp += a;
  }
  const float rp = os * __builtin_amdgcn_rcpf(xadd(sp));
#pragma unroll
  for (int i = 0; i < 16; ++i) p[i] *= rp;
  pack16(p, p0, p1);
}
DI unsigned umax4(const u32x4& x) { const unsigned a = x[0] > x[1] ? x[0] : x[1], c = x[2] > x[3] ? x[2] : x[3]; return a > c ? a : c; }

DI void ssq_step(const u32x4& x, float& s0, float& s1) {
#pragma unroll
  for (int i = 0; i < 4; i += 2) {
    bf16x2 a = __builtin_bit_cast(bf16x2, (unsigned)x[i]), c = __builtin_bit_cast(bf16x2, (unsigned)x[i + 1]);
    s0 = __builtin_amdgcn_fdot2_f32_bf16(a, a, s0, false);
    s1 = __builtin_amdgcn_fdot2_f32_bf16(c, c, s1, false);
  }
}

DI void sum_step(const u32x4& x, float& s0, float& s1) {
  const bf16x2 on = __builtin_bit_cast(bf16x2, 0x3F803F80u);
#pragma unroll
  for (int i = 0; i < 4; i += 2) {
    s0 = __builtin_amdgcn_fdot2_f32_bf16(__builtin_bit_cast(bf16x2, (unsigned)x[i]), on, s0, false);
    s1 = __builtin_amdgcn_fdot2_f32_bf16(__builtin_bit_cast(bf16x2, (unsigned)x[i + 1]), on, s1, false);
  }
}
DI u32x2 cs_split(float cs, unsigned biasbits) {
  const float hi = rb(cs), lo = rb(cs - hi), l2 = cs - hi - lo;
  u32x2 f;
  f[0] = pk2(hi, lo);
  f[1] = (pk2(l2, 0.f) & 0xffffu) | biasbits;
  return f;
}
DI u32x4 wide(u32x2 f) { u32x4 o; o[0] = f[0]; o[1] = f[1]; o[2] = 0u; o[3] = 0u; return o; }

DI constexpr int unit_f0(int u) { const int p = u / 7, i = u - 7 * p; return 56 * p + (int)((0x142C201C100400ull >> (8 * i)) & 0xFFu); }
DI constexpr int unit_f1(int u) { const int p = u / 7, i = u - 7 * p; return 56 * p + (int)((0x30342824180C08ull >> (8 * i)) & 0xFFu); }

struct Params {
  const void* x; const void* wqkv; const void* wphi; const void* wo;
  const void* w1; const void* b1; const void* w2; const void* b2;
  void* out; void* ws;
};

DI void wait_flags(const GAS u64* p, u64 fv, unsigned msk = 48u) {
  bool open = (threadIdx.x & msk) == 0;
#pragma unroll 1
  for (unsigned sp = 0; sp < SPIN_CAP; ++sp) {
    if (open) open = __hip_atomic_load(p, __ATOMIC_RELAXED, __HIP_MEMORY_SCOPE_AGENT) != fv;
    if (__builtin_amdgcn_ballot_w64(open) == 0ull) break;
    __builtin_amdgcn_s_sleep(PSL);
  }
}

__global__ void __launch_bounds__(512, 2) fwd(Params P) {
  __shared__ __attribute__((aligned(16))) char smem[L_END];
  const unsigned lb = (unsigned)(size_t)(LAS char*)smem;
  int tid = threadIdx.x, lane = tid & 63, r = lane & 31;
  bool hh = lane >= 32;
  int w = __builtin_amdgcn_readfirstlane(tid >> 6);
  int st = w & 3, kh = w >> 2;
  int b = blockIdx.x;
  int t = (b & 7) * 2 + (b >> 7), j = (b >> 3) & 15;
  int g = t * 4 + ((st + ROT * j) & 3);

  GAS char* wsg = (GAS char*)P.ws;
  auto rs_ws = __builtin_amdgcn_make_buffer_rsrc(P.ws, 0, WS_END, 0x00020000);
  u64 E, FV = 0;
  asm volatile("buffer_load_dwordx2 %0, off, %1, 0 sc1" : "=v"(E) : "s"(rs_ws) : "memory");
  auto rs_x = __builtin_amdgcn_make_buffer_rsrc((void*)P.x, 0, 2048 * 448 * 2, 0x00020000);
  auto rs_out = __builtin_amdgcn_make_buffer_rsrc(P.out, 0, 2048 * 448 * 2, 0x00020000);
  auto rs_wo = __builtin_amdgcn_make_buffer_rsrc((void*)P.wo, 0, 448 * 448 * 2, 0x00020000);
  auto rs_w1 = __builtin_amdgcn_make_buffer_rsrc((void*)P.w1, 0, 448 * 1792 * 2, 0x00020000);
  auto rs_w2 = __builtin_amdgcn_make_buffer_rsrc((void*)P.w2, 0, 1792 * 448 * 2, 0x00020000);
  auto rs_wq = __builtin_amdgcn_make_buffer_rsrc((void*)P.wqkv, 0, 448 * 1344 * 2, 0x00020000);

  unsigned lane16 = (unsigned)lane * 16u;
  unsigned wl = lb + lane16;
  unsigned kofs = (unsigned)kh * 14336u + lane16;
  int rx = r ^ (kh * 16);
  auto relane = [&]() {
    asm volatile("" : "+v"(tid));
    asm volatile("" : "+s"(w));
    asm volatile("" : "+s"(b));
    lane = tid & 63; r = lane & 31; hh = lane >= 32;
    st = w & 3; kh = w >> 2;
    t = (b & 7) * 2 + (b >> 7); j = (b >> 3) & 15; g = t * 4 + ((st + ROT * j) & 3);
    lane16 = (unsigned)lane * 16u; wl = lb + lane16; kofs = (unsigned)kh * 14336u + lane16;
    rx = r ^ (kh * 16);
  };

  u32x4 xf[14];
  u32x4 hc[12];
  u32x2 hx;
  u32x4 hw[12];
  u32x4 he[2];
  int stn_ = 0; (void)stn_;
  unsigned stv_ = 0; (void)stv_;

  auto fill_c = [&](auto fc, unsigned slot) {
    constexpr int f = decltype(fc)::v;
    const unsigned a = wl + L_R0 + slot * SLOT + (unsigned)w * 3072u;
#pragma unroll
    for (int i = 0; i < 3; ++i) sts128(a + i * 1024, hc[3 * f + i]);
    if (w == f) *(LAS u32x2*)(lb + L_R0 + slot * SLOT + 24576u + (unsigned)lane * 8u) = hx;
  };
  auto fill_w = [&](auto fc, unsigned slot) {
    constexpr int f = decltype(fc)::v;
    const unsigned a = wl + L_R0 + slot * SLOT;
#pragma unroll
    for (int i = 0; i < 3; ++i) sts128(a + (unsigned)w * 3072u + i * 1024, hw[3 * f + i]);
    if (kh == (f & 1)) sts128(a + 24576u + (unsigned)st * 1024u, he[f >> 1]);
  };
  auto store_unit = [&](unsigned base, int kk, u32x4 v) {
    const unsigned u0 = base + (unsigned)(7 * (j >> 1) + 3 * (j & 1)) * 512u;
    if (kk == 0) st128(v, rs_ws, lane16, u0, 16);
    else if (!hh) st128(v, rs_ws, lane16, u0 + 1024u, 16);
    else {
      u32x2 h2; h2[0] = v[0]; h2[1] = v[1];
      __builtin_amdgcn_raw_buffer_store_b64(h2, rs_ws, lane16, base + (unsigned)((7 * (j >> 1) + 5) * 512 + (j & 1) * 8), 16);
    }
  };
  auto refetch = [&](u32x4& x, unsigned vo, unsigned so, unsigned lim) {
    if (__builtin_expect(__builtin_amdgcn_ballot_w64(umax4(x) > lim) != 0ull, 0)) {
#pragma unroll 1
      for (unsigned sp = 0; sp < SPIN_CAP; ++sp) {
        x = __builtin_amdgcn_raw_buffer_load_b128(rs_ws, vo, so, 16);
        CNT();
        if (__builtin_amdgcn_ballot_w64(umax4(x) > lim) == 0ull) break;
        __builtin_amdgcn_s_sleep(1);
      }
    }
  };
  auto load_x14 = [&](unsigned base, auto slc) {
    if constexpr (decltype(slc)::v > 0) __builtin_amdgcn_s_sleep(decltype(slc)::v);
    ST();
#pragma unroll
    for (int s = 0; s < 14; ++s) xf[s] = __builtin_amdgcn_raw_buffer_load_b128(rs_ws, base + kofs, (unsigned)(s * 1024), 16);
    SCHED();
  };
  auto refetch_x = [&](auto ic, unsigned vo, unsigned lim) {
    constexpr int s = decltype(ic)::v;
    if (__builtin_expect(__builtin_amdgcn_ballot_w64(umax4(xf[s]) > lim) != 0ull, 0)) {
#pragma unroll 1
      for (unsigned sp = 0; sp < SPIN_CAP; ++sp) {
        xf[s] = __builtin_amdgcn_raw_buffer_load_b128(rs_ws, vo, (unsigned)(s * 1024), 16);
        CNT();
        if (__builtin_amdgcn_ballot_w64(umax4(xf[s]) > lim) == 0ull) break;
        __builtin_amdgcn_s_sleep(1);
      }
    }
  };
  auto h_off = [&](int c) {
    const int sx = c == 3 ? (j >> 1) : 8 + 16 * c + j, kx = c == 3 ? (j & 1) : kh;
    const int f = sx / 14;
    return (unsigned)(14 * f + 14 * kx + sx) * 1024u;
  };
  auto store_h = [&](int c, u32x4 v) {
    if (c != 3 || !kh) st128(v, rs_ws, lane16, WS_H + (unsigned)g * 114688u + h_off(c), 16);
  };
  auto set_flag = [&](int ly, int fidx) {
    u64 f = FV;
    asm volatile("" : "+s"(f));
    if (tid == 0) __hip_atomic_store(FK(ly, fidx), f, __ATOMIC_RELAXED, __HIP_MEMORY_SCOPE_AGENT);
  };
  auto mksent = [&]() { unsigned m; asm volatile("v_mov_b32 %0, -1" : "=v"(m)); u32x4 v; v[0] = v[1] = v[2] = v[3] = m; return v; };
  auto cs_kv = [&](int idx, unsigned biasbits) {
    const f32x4 c = ldsf4(lb + L_CP + (unsigned)(idx * 448 + r * 16));
    return wide(cs_split(r < 28 ? (c[0] + c[1]) + (c[2] + c[3]) : 0.f, biasbits));
  };
  auto cf_frag = [&](int idx, int row) { return wide(*(LAS u32x2*)(lb + L_CF + (unsigned)(idx * 256 + row * 8))); };
  auto tr_off = [&]() { return (unsigned)((hh ? 512 : 0) + ((lane >> 2) & 3) * 64 + ((lane >> 4) & 1) * 32 + (lane & 3) * 8); };
  auto xsend = [&](const f32x16& a) {
    const unsigned ad = wl + L_X + (unsigned)w * 2048u;
    f32x4 x, y;
#pragma unroll
    for (int i = 0; i < 4; ++i) { x[i] = a[8 + i]; y[i] = a[12 + i]; }
    stsf4(ad, x); stsf4(ad + 1024u, y);
  };
  auto xrecv = [&]() {
    const unsigned ad = wl + L_X + (unsigned)(w ^ 4) * 2048u;
    f32x4 x = ldsf4(ad), y = ldsf4(ad + 1024u);
    f32x8 o;
#pragma unroll
    for (int i = 0; i < 4; ++i) { o[i] = x[i]; o[4 + i] = y[i]; }
    return o;
  };
  auto stat_send = [&](unsigned base, float ssq, float sum) {
    f32x2 v; v[0] = ssq; v[1] = sum;
    if (!hh) *(LAS f32x2*)(lb + base + (unsigned)w * 256u + (unsigned)r * 8u) = v;
  };
  auto stat_recv = [&](unsigned base, float ssq, float sum, float& rs, float& sb, u32x4& stf) {
    const f32x2 v = *(LAS f32x2*)(lb + base + (unsigned)(w ^ 4) * 256u + (unsigned)r * 8u);
    ssq += v[0]; sum += v[1];
    const float mean = sum * (1.0f / 448.0f);
    float var = ssq * (1.0f / 448.0f) - mean * mean;
    var = fmaxf(var, 0.f);
    sb = rb(HQ ? __builtin_amdgcn_sqrtf(rb(rb(var) + 1e-5f)) : __builtin_sqrtf(rb(rb(var) + 1e-5f)));
    rs = HQ ? __builtin_amdgcn_rcpf(sb) : 1.0f / sb;
    stf[0] = hh ? 0u : pk2(-mean, -mean);
    stf[1] = hh ? 0u : pk2(-mean, sb);
    stf[2] = 0u; stf[3] = 0u;
  };
  auto dma1 = [&](decltype(rs_ws) rsrc, unsigned voff, unsigned soff, unsigned dst) {
    unsigned keep;
    asm volatile("s_mov_b32 %0, m0\n\ts_mov_b32 m0, %1\n\ts_nop 0\n\t"
                 "buffer_load_dwordx4 %2, %3, %4 offen lds\n\t"
                 "s_mov_b32 m0, %0"
                 : "=&s"(keep) : "s"(dst), "v"(voff), "s"(rsrc), "s"(soff) : "memory");
  };
  auto dma1s = [&](unsigned soff, unsigned dst) {
    unsigned keep;
    asm volatile("s_mov_b32 %0, m0\n\ts_mov_b32 m0, %1\n\ts_nop 0\n\t"
                 "buffer_load_dwordx4 %2, %3, %4 offen sc1 lds\n\t"
                 "s_mov_b32 m0, %0"
                 : "=&s"(keep) : "s"(dst), "v"(lane16), "s"(rs_ws), "s"(soff) : "memory");
  };
  auto bf_load = [&](int img, int fi, unsigned dst) {
    const int tile = fi / 28, s = fi - 28 * tile;
    const int q = lane >> 2, hq = q >> 3;
    int row;
    if (img == 2) row = 16 * fi + 4 * hq + 8 * ((q >> 2) & 1) + (q & 3);
    else { const int u = 2 * s + hq; row = ((q >> 2) & 1 ? unit_f1(u) : unit_f0(u)) + (q & 3); }
    const unsigned ch = (unsigned)(lane & 3) * 16u;
    const unsigned jo = ((unsigned)j * 56u) & ~15u;
    if (img == 0) {
      if (tile < 3) dma1(rs_wq, (unsigned)row * 2688u + ch, jo + (tile == 0 ? 896u : (tile == 1 ? 1792u : 0u)), dst);
      else dma1(rs_wo, (unsigned)row * 896u + ch, jo, dst);
    }
    else if (img == 1) dma1(rs_w1, (unsigned)row * 3584u + ch, (unsigned)j * 224u + (unsigned)((tile + 3) & 3) * 64u - (tile == 0 ? 32u : 0u), dst);
    else dma1(rs_w2, (unsigned)row * 896u + ch, jo, dst);
  };
  auto bf_store = [&](int img, int fi, unsigned src) {
    const int tile = fi / 28;
    int vw = 28, oc = -1, rows = 28; unsigned dst, sh = (unsigned)(j & 1) * 8u;
    if (img == 0) { oc = tile == 0 ? 28 : -1; rows = tile < 3 ? 29 : 28; dst = tile < 3 ? (unsigned)(fi * 928) : G_WO + (unsigned)((fi - 84) * 896); }
    else if (img == 1) { vw = tile == 0 ? 16 : 32; oc = tile == 0 ? 16 : -1; rows = 32; dst = G_W1 + (unsigned)(fi * 1024); sh = tile == 0 ? 32u : 0u; }
    else dst = G_WO + T28 + (unsigned)(fi * 896);
    const unsigned a = src + sh + (unsigned)((hh ? 512 : 0) + ((lane >> 2) & 3) * 64 + ((lane >> 4) & 1) * 32 + (lane & 3) * 8);
    const u32x2 x0 = trr(a), x1 = trr(a + 256u);
    u32x4 o;
    const unsigned padv = (r == oc) ? 0x3F803F80u : 0u;
    o[0] = r < vw ? x0[0] : padv; o[1] = r < vw ? x0[1] : padv; o[2] = r < vw ? x1[0] : padv; o[3] = r < vw ? x1[1] : padv;
    const bool wr = r < rows || (hh && img == 0 && fi == 83);
    if (wr) st128(o, rs_ws, WS_WT + (unsigned)j * G_STRIDE + dst + (unsigned)(((hh ? rows : 0) + r) * 16), 0, 16);
    if (img == 1 || (img == 0 && tile < 3)) {
      const bf16x2 on = __builtin_bit_cast(bf16x2, 0x3F803F80u);
      float sum = 0.f;
#pragma unroll
      for (int i = 0; i < 4; ++i) sum = __builtin_amdgcn_fdot2_f32_bf16(__builtin_bit_cast(bf16x2, (unsigned)o[i]), on, sum, false);
      sum = xadd(sum);
      u32x4 q;
      q[0] = f2u(sum);
      q[1] = (unsigned)__builtin_amdgcn_mov_dpp((int)q[0], 0x55, 0xf, 0xf, false);
      q[2] = (unsigned)__builtin_amdgcn_mov_dpp((int)q[0], 0xAA, 0xf, 0xf, false);
      q[3] = (unsigned)__builtin_amdgcn_mov_dpp((int)q[0], 0xFF, 0xf, 0xf, false);
      if ((lane & 35) == 0) st128(q, rs_ws, WS_WT + (unsigned)j * G_STRIDE + G_CS + (unsigned)((img * 84 + fi) * 128 + r * 4), 0, 16);
    }
  };

  auto colsum_ld = [&](u32x4* c, u32x2& bv) {
    auto rs_b1 = __builtin_amdgcn_make_buffer_rsrc((void*)P.b1, 0, 1792 * 2, 0x00020000);
#pragma unroll
    for (int i = 0; i < 4; ++i)
      ld_dev(c[i], rs_ws, (i == 3 && hh) ? 0u : lane16, WS_WT + (unsigned)j * G_STRIDE + G_CS + (unsigned)(w * 3584 + i * 1024));
    asm volatile("s_nop 4\n\tbuffer_load_dwordx2 %0, %1, %2, %3 offen" : "=v"(bv)
                 : "v"((unsigned)(lane & 7) * 8u), "s"(rs_b1), "s"(w == 2 ? 4096u : (unsigned)(j * 224 + (w & 3) * 64)) : "memory");
  };
  auto colsum = [&](const u32x4* cu, const u32x2& bv) {
    u32x2 cf[4];
    f32x4 c[4];
#pragma unroll
    for (int i = 0; i < 4; ++i) c[i] = __builtin_bit_cast(f32x4, cu[i]);
#pragma unroll
    for (int e = 0; e < 4; ++e) {
      float v = c[0][e] + c[1][e] + c[2][e] + (hh ? 0.f : c[3][e]);
      v += __builtin_bit_cast(float, __builtin_amdgcn_mov_dpp(__builtin_bit_cast(int, v), 0x128, 0xf, 0xf, false));
      auto x = __builtin_amdgcn_permlane16_swap(f2u(v), f2u(v), false, false);
      v = xadd(__builtin_bit_cast(float, (unsigned)x[0]) + __builtin_bit_cast(float, (unsigned)x[1]));
      cf[e] = cs_split(v, (e & 1) ? bv[e >> 1] & 0xffff0000u : bv[e >> 1] << 16);
    }
    if (lane < 8) {
      const unsigned a = lb + L_CF + (unsigned)((w - 2) * 256 + lane * 32);
      u32x4 o; o[0] = cf[0][0]; o[1] = cf[0][1]; o[2] = cf[1][0]; o[3] = cf[1][1];
      sts128(a, o);
      o[0] = cf[2][0]; o[1] = cf[2][1]; o[2] = cf[3][0]; o[3] = cf[3][1];
      sts128(a + 16u, o);
    }
  };

  RT(0);
  ST();
  const unsigned bst = lb + L_XS + (unsigned)w * 7680u, bsr = lb + L_Q + (unsigned)w * 3072u;
  const int f0i = 56 + 4 * t + w - 3;
  const bool has0 = w >= 3 && w < 7 && t < 14;
  const int f1i = ((7 * t + w) & 3) * 28 + ((7 * t + w) >> 2);
  int f2i;
  { const int sx = w == 6 ? (t >> 1) : 8 + 16 * (w >> 1) + t, kx = w == 6 ? (t & 1) : (w & 1); f2i = 14 * (sx / 14) + 14 * kx + sx; }
  auto l2p = [&]() {
    const int c = (t & 1) * 16 + j, x = t >> 1;
    auto pf = [&](decltype(rs_ws) rsrc, int n, unsigned vo, unsigned so) {
      unsigned keep;
      asm volatile("s_mov_b32 %0, m0\n\ts_mov_b32 m0, %1\n\ts_nop 0\n\t"
                   "buffer_load_dword %2, %3, %4 offen lds\n\t"
                   "s_mov_b32 m0, %0"
                   : "=&s"(keep) : "s"(bst), "v"(lane < n ? vo : 0x40000000u), "s"(rsrc), "s"(so) : "memory");
    };
    auto grow = [&](int sk, int gq) { const int u = 2 * sk + (gq >> 1); return (gq & 1) ? unit_f1(u) : unit_f0(u); };
    const int wp = (w + PFO) & 7;
    if (L2P >= 3 && wp < 3) {
      if (wp == 0) {
        u64 pc; unsigned len;
        asm volatile("s_getpc_b64 %0\n.Lfwd_a:\n\ts_mov_b32 %1, .Lfwd_z-.Lfwd_a" : "=s"(pc), "=s"(len));
        auto rs_c = __builtin_amdgcn_make_buffer_rsrc((void*)pc, 0, (int)len, 0x00020000);
        pf(rs_c, CT, (unsigned)(lane + c * CT) * 128u, 0u);
      }
    }
    else if (wp == 0) {
      const int fq = 8 * x + (c >> 2);
      if (fq < 28) pf(rs_wq, 28, (unsigned)((lane / 7) * 2688 + (lane % 7) * 128), (unsigned)grow(fq, c & 3) * 2688u);
      else if (fq < 56) pf(rs_wo, 28, (unsigned)lane * 128u, (unsigned)grow(fq - 28, c & 3) * 896u);
    }
    else if (wp == 1) pf(rs_w1, 56, (unsigned)lane * 128u, (unsigned)(grow(((7 * x) >> 1) + (c >> 3), (c >> 1) & 3) + 2 * (c & 1)) * 3584u);
    else if (wp == 2) pf(rs_w2, 49, (unsigned)lane * 128u, (unsigned)(32 * x + c) * 6272u);
    else if (wp < 7) { const int i = (wp - 3) * 49 + lane; pf(rs_wq, 49, (unsigned)((i / 14) * 2688 + (i % 14) * 128), (unsigned)(14 * c * 2688 + 896)); }
    else pf(rs_x, 56, (unsigned)lane * 128u, (unsigned)((t * 128 + 8 * j) * 896));
    SCHED();
  };
  l2p();
  if (has0) bf_load(0, f0i, bsr);
  if (w < 7) { bf_load(1, f1i, bsr + 1024u); bf_load(2, 7 * t + w, bsr + 2048u); }
  else {
    {
      const int q = lane >> 4, l = lane & 15;
      const GAS u64* fp = q == 0 ? FI(j * 16 + l) : (q == 3 ? FT(t * 16 + l) : FK(q - 1, j * 16 + l));
      unsigned keep;
      asm volatile("s_mov_b32 %0, m0\n\ts_mov_b32 m0, %1\n\ts_nop 0\n\t"
                   "buffer_load_dword %2, %3, 0 offen sc1 lds\n\t"
                   "s_mov_b32 m0, %0"
                   : "=&s"(keep) : "s"(lb + L_RAW + 3072u), "v"((unsigned)((const GAS char*)fp - wsg)), "s"(rs_ws) : "memory");
    }
    auto rs_ph = __builtin_amdgcn_make_buffer_rsrc((void*)P.wphi, 0, 16 * 784 * 2, 0x00020000);
    auto rs_b2 = __builtin_amdgcn_make_buffer_rsrc((void*)P.b2, 0, 448 * 2, 0x00020000);
    dma1(rs_ph, lane16, (unsigned)j * 1568u, lb + L_RAW);
    dma1(rs_ph, lane < 34 ? lane16 + 1024u : 0u, (unsigned)j * 1568u, lb + L_RAW + 1024u);
    dma1(rs_b2, lane < 4 ? lane16 : 0u, (unsigned)j * 56u - (j == 15 ? 8u : 0u), lb + L_RAW + 2048u);
    u32x4 z; z[0] = z[1] = z[2] = z[3] = 0u;
    if (lane == 0) sts128(lb + L_CNT, z);
  }
  u32x4 xr;
  BAR();
  {
    const unsigned so = (unsigned)st * (112u * 2688u) + (((unsigned)j * 56u) & ~15u) + (kh ? 1792u : 896u);
    const unsigned vb = (unsigned)(((lane >> 2) & 3) * 2688 + (lane & 3) * 16), sh = (unsigned)(lane >> 4) * 8u;
    sfor<7>([&](auto ic) {
      constexpr int i = decltype(ic)::v;
      constexpr unsigned C = (unsigned)unit_f0(2 * i) | ((unsigned)unit_f1(2 * i) << 8) | ((unsigned)unit_f0(2 * i + 1) << 16) | ((unsigned)unit_f1(2 * i + 1) << 24);
      dma1(rs_wq, ((C >> sh) & 0xFFu) * 2688u + vb, so, lb + L_K + (unsigned)(7 * w + i) * 1024u);
    });
  }
  SCHED();
  ST();
  asm volatile("s_waitcnt vmcnt(7)" : "+v"(E) :: "memory");
  ST();
  if (has0) bf_store(0, f0i, bsr);
  if (w < 7) { bf_store(1, f1i, bsr + 1024u); bf_store(2, f2i, bsr + 2048u); }
  else {
    u32x4 z; z[0] = z[1] = z[2] = z[3] = 0u;
    st128(z, rs_ws, WS_KV + (unsigned)(((hh ? 16 : 0) + j) * 8192 + t * 512 + r * 16), 0, 16);
  }
  SCHED();
  ST();
  {
    u32x4 raw[14];
    unsigned la[7], go[7];
    {
      int row = lane / 14, col = lane - row * 14;
#pragma unroll
      for (int i = 0; i < 7; ++i) {
        go[i] = (unsigned)((g * 32 + row) * 896 + kh * 448 + col * 16);
        asm volatile("buffer_load_dwordx4 %0, %1, %2, 0 offen" : "=v"(raw[i]) : "v"(go[i]), "s"(rs_x) : "memory");
        la[i] = (unsigned)(row * 240 + col * 16);
        col += 8; row += 4;
        if (col >= 14) { col -= 14; row += 1; }
      }
#pragma unroll
      for (int i = 0; i < 7; ++i)
        asm volatile("buffer_load_dwordx4 %0, %1, %2, 0 offen offset:224" : "=v"(raw[7 + i]) : "v"(go[i]), "s"(rs_x) : "memory");
    }
    {
      const unsigned rowb = (unsigned)(((g * 32 + r) * 448 + 28 * j + (hh ? 4 : 0) + 16 * kh) * 2);
      const u32x2 a = __builtin_amdgcn_raw_buffer_load_b64(rs_x, rowb, 0, 0);
      const u32x2 c = __builtin_amdgcn_raw_buffer_load_b64(rs_x, rowb, 16, 0);
      xr[0] = a[0]; xr[1] = a[1]; xr[2] = c[0]; xr[3] = c[1];
    }
    SCHED();
    ST();
    E = (u64)__builtin_amdgcn_readfirstlane((unsigned)E) | ((u64)__builtin_amdgcn_readfirstlane((unsigned)(E >> 32)) << 32);
    FV = (E + 1ull) * 0x9E3779B97F4A7C15ull + 0x632BE59BD9B4E019ull;
    if (w == 7) {
      const unsigned pa = lb + L_RAW + (unsigned)(r < 28 ? r : 0) * 2u;
      unsigned tv[16];
#pragma unroll
      for (int e = 0; e < 16; ++e) {
        const int d0 = 16 * (e >> 3) + 8 * ((e >> 2) & 1) + (e & 3);
        const int d = d0 + (hh ? 4 : 0);
        tv[e] = *(LAS unsigned short*)(pa + (unsigned)((d < 28 ? d : 0) * 56));
      }
      const unsigned v2 = *(LAS unsigned short*)(pa + 2048u + (j == 15 ? 8u : 0u));
#pragma unroll
      for (int i = 0; i < 2; ++i) {
        u32x4 o;
#pragma unroll
        for (int e = 0; e < 4; ++e) {
          const bool ok = r < 28 && !(hh && i == 1 && e >= 2);
          o[e] = ok ? (tv[8 * i + 2 * e] | (tv[8 * i + 2 * e + 1] << 16)) : 0u;
        }
        sts128(wl + L_WPHI + (unsigned)i * 1024u, o);
      }
      if (lane < 32) *(LAS float*)(lb + L_B2 + (unsigned)lane * 4u) = r < 28 ? bflo(v2) : 0.f;
      asm volatile("s_waitcnt vmcnt(17)" ::: "memory");
    } else {
      if (w == 0) {
        u32x4 v;
#pragma unroll
        for (int e = 0; e < 8; e += 2) {
          const int d0 = 8 * (e >> 2) + (hh ? 4 : 0) + (e & 3);
          v[e >> 1] = (d0 == r ? 0x3F80u : 0u) | (d0 + 1 == r ? 0x3F800000u : 0u);
        }
        sts128(wl + L_ID, v);
      }
      asm volatile("s_waitcnt vmcnt(19)" ::: "memory");
    }
    SCHED();
    {
      const unsigned kb = lb + L_K + (unsigned)w * 7168u;
      const unsigned a = kb + (unsigned)(j & 1) * 8u + tr_off();
      const unsigned padv = (r == 28 && kh == 0) ? 0x3F803F80u : 0u;
      float s0 = 0.f, s1 = 0.f;
      u32x4 f[7];
#pragma unroll
      for (int i = 0; i < 7; ++i) f[i] = ldt(a + i * 1024);
#pragma unroll
      for (int i = 0; i < 7; ++i) {
#pragma unroll
        for (int e = 0; e < 4; ++e) f[i][e] = r < 28 ? f[i][e] : padv;
        sts128(kb + lane16 + i * 1024, f[i]);
        sum_step(f[i], s0, s1);
      }
      s0 = xadd(s0 + s1);
      if (lane < 28) *(LAS float*)(lb + L_CP + (unsigned)(kh * 448 + r * 16 + st * 4)) = s0;
    }
    SCHED();
    ST();
    const unsigned ra = bst + (unsigned)r * 240u;
    sfor<2>([&](auto hc_) {
      constexpr int h = decltype(hc_)::v;
      if constexpr (h == 0) {
        asm volatile("s_waitcnt vmcnt(9)" : "+v"(raw[0]), "+v"(raw[1]), "+v"(raw[2]), "+v"(raw[3]), "+v"(raw[4]), "+v"(raw[5]), "+v"(raw[6]) :: "memory");
        if (lane == 0) {
          const unsigned o = __hip_atomic_fetch_add((LAS unsigned*)(lb + L_CNT), 1u, __ATOMIC_RELAXED, __HIP_MEMORY_SCOPE_WORKGROUP);
          if (o == 7u) __hip_atomic_store(FI(j * 16 + t), FV, __ATOMIC_RELAXED, __HIP_MEMORY_SCOPE_AGENT);
        }
      } else
        asm volatile("s_waitcnt vmcnt(2)" : "+v"(raw[7]), "+v"(raw[8]), "+v"(raw[9]), "+v"(raw[10]), "+v"(raw[11]), "+v"(raw[12]), "+v"(raw[13]) :: "memory");
#pragma unroll
      for (int i = 0; i < 7; ++i) sts128(bst + la[i], raw[7 * h + i]);
      sfor<7>([&](auto ic) {
        constexpr int s = decltype(ic)::v;
        const u32x2 a = *(LAS u32x2*)(ra + (unsigned)(hh ? 2 * unit_f0(2 * s + 1) : 2 * unit_f0(2 * s)));
        const u32x2 c = *(LAS u32x2*)(ra + (unsigned)(hh ? 2 * unit_f1(2 * s + 1) : 2 * unit_f1(2 * s)));
        xf[7 * h + s][0] = a[0]; xf[7 * h + s][1] = a[1]; xf[7 * h + s][2] = c[0]; xf[7 * h + s][3] = c[1];
      });
    });
    SCHED();
  }
  ST();
  BAR();
#pragma unroll
  for (int i = 0; i < 12; ++i) asm volatile("" : "=v"(hc[i]), "=v"(hw[i]));
  asm volatile("" : "=v"(hx), "=v"(he[0]), "=v"(he[1]));

  for (int layer = 0; layer < 2; ++layer) {
    relane();
    ST();
    if (layer == 1) {
      if (SL1 > 0) __builtin_amdgcn_s_sleep(SL1);
      ST();
      const unsigned vo = WS_X2 + (unsigned)g * 28672u + kofs;
      sfor<14>([&](auto ic) {
        constexpr int s = decltype(ic)::v;
        ld_dev(xf[s], rs_ws, vo, (unsigned)(s * 1024));
      });
      SCHED();
    }
    float rs, sb; u32x4 stf;
    u64 fbv;
    auto p2flags = [&]() {
      return (const GAS u64*)((layer == 0 && (lane & 16)) ? FT(t * 16 + (lane & 15)) : FK(layer, j * 16 + (lane & 15))); };
    auto bg = [&](auto ic) {
      constexpr int i = decltype(ic)::v;
      if (layer == 0) {
        const unsigned ib = WS_WT + (unsigned)j * G_STRIDE;
        if constexpr (i < 14) {
          constexpr int m = i / 7, q = i % 7;
          if constexpr (q == 3) he[m] = __builtin_amdgcn_raw_buffer_load_b128(rs_ws, lane16, ib + G_W1 + (unsigned)(2 * m + kh) * SLOT + (unsigned)(24 + st) * 1024u, 16);
          else { constexpr int x = 6 * m + (q < 3 ? q : q - 1);
            hw[x] = __builtin_amdgcn_raw_buffer_load_b128(rs_ws, lane16, ib + G_W1 + (unsigned)w * 3072u + (x / 3) * SLOT + (x % 3) * 1024, 16); }
        } else if constexpr (i == 14) hx = __builtin_amdgcn_raw_buffer_load_b64(rs_ws, (unsigned)lane * 8u, ib + G_WO + T28 + (unsigned)st * T28 + 24576u, 16);
        else { constexpr int x = i - 15;
          hc[x] = __builtin_amdgcn_raw_buffer_load_b128(rs_ws, lane16, ib + G_WO + T28 + (unsigned)w * 3072u + (x / 3) * T28 + (x % 3) * 1024, 16); }
      }
      SCHED();
    };
    auto bgs = [&](auto oc, auto nc, auto mc, auto kc) {
      constexpr int o = decltype(oc)::v, n = decltype(nc)::v, m = decltype(mc)::v, k = decltype(kc)::v;
      constexpr int lo = k * n / m, hi = (k + 1) * n / m;
      sfor<hi - lo>([&](auto ic) { bg(IC<o + lo + decltype(ic)::v>{}); });
    };
#define BGS(o, n, m, k) bgs(IC<(o)>{}, IC<(n)>{}, IC<(m)>{}, IC<(k)>{})
#define OB NA
#define OC (OB + NB)
#define OD (OC + NC)
#define OE (OD + ND)
#define OF (OE + NE)
#define OG (OF + NF)
#define OH (OG + NG)
    static_assert(OH + NH == 27 && OE >= 4 && OF >= 7 && OG >= 11 && OH >= 14 && OC <= 14, "");
    {
      unsigned wq = wl + L_K + (unsigned)kh * 14336u;
      asm volatile("" : "+v"(wq));
      const GAS u64* fbp = FI(j * 16 + (lane & 15));
      asm volatile("" : "=v"(fbv));
      if (layer == 0) asm volatile("global_load_dwordx2 %0, %1, off sc1" : "=v"(fbv) : "v"(fbp) : "memory");
      float sq0 = 0.f, sq1 = 0.f, sq2 = 0.f, sq3 = 0.f;
      f32x16 ak = zero16(), av = zero16();
      {
        const unsigned lim = layer ? SENT - 1u : SENT;
        const unsigned vo = WS_X2 + (unsigned)g * 28672u + kofs;
        u32x4 wk[14], wv[14];
        SCHED();
#pragma unroll
        for (int s = 0; s < KP; ++s) { wk[s] = lds128(wq + s * 1024); wv[s] = lds128(wq + SLOT + s * 1024); }
        SCHED();
        sfor<14>([&](auto ic) {
          constexpr int s = decltype(ic)::v;
          if constexpr (s + KP < 14) { wk[s + KP] = lds128(wq + (s + KP) * 1024); wv[s + KP] = lds128(wq + SLOT + (s + KP) * 1024); }
          wait_vm<13 - s>(xf[s], layer);
          refetch_x(ic, vo, lim);
          ak = MFMA(wk[s], xf[s], ak);
          av = MFMA(xf[s], wv[s], av);
          if (Q4 && (s & 1)) ssq_step(xf[s], sq2, sq3); else ssq_step(xf[s], sq0, sq1);
          SCHED();
          if constexpr (s == S0) {
            if (layer == 0) {
              wait_u64(fbv);
              if (__builtin_amdgcn_ballot_w64(fbv != FV) != 0ull) wait_flags(fbp, FV);
              const unsigned so = WS_WT + (unsigned)j * G_STRIDE + 2u * T29;
#pragma unroll
              for (int i = 0; i < 3; ++i) dma1s(so + (unsigned)(3 * w + i) * 1024u, lb + L_Q + (unsigned)(3 * w + i) * 1024u);
              if (w == 0) dma1s(so + 24576u, lb + L_Q + 24576u);
              if (w == 1 && lane < 27) dma1s(so + 25600u, lb + L_Q + 25600u);
              if (w >= 2 && w < 7) colsum_ld(hc + 8, hx);
            }
            SCHED();
          }
          if constexpr (s > S0) BGS(0, NA, 13 - S0, s - S0 - 1);
        });
      }
      CST(layer * 8 + 0);
      st16(wl + L_R0 + (unsigned)w * 4096u, ak);
      {
        const unsigned ad = wl + L_X + (unsigned)w * 2048u;
        f32x4 x, y;
        if (kh) {
#pragma unroll
          for (int i = 0; i < 4; ++i) { x[i] = av[i]; y[i] = av[4 + i]; }
          stsf4(ad, x); stsf4(ad + 1024u, y);
        } else {
#pragma unroll
          for (int i = 0; i < 4; ++i) { x[i] = av[8 + i]; y[i] = av[12 + i]; }
          stsf4(ad, x); stsf4(ad + 1024u, y);
        }
      }
      const float ssq = xadd(Q4 ? (sq0 + sq1) + (sq2 + sq3) : sq0 + sq1), sum = xadd(hh ? ak[12] : 0.f);
      stat_send(L_R0 + 32768u, ssq, sum);
      ST();
      BAR();
      BGS(OB, NB, 4, 0);
      const unsigned pk_ = wl + L_R0 + (unsigned)(w ^ 4) * 4096u;
      add16f(pk_, ak, [&]() { stat_recv(L_R0 + 32768u, ssq, sum, rs, sb, stf); });
      ak = MFMA(cs_kv(0, 0u), stf, ak);
      av = MFMA(stf, cs_kv(1, r == 28 ? 0x3F800000u : 0u), av);
      SCHED();
      BGS(OB, NB, 4, 1);
#if !(AF & 1)
      add16(pk_, ak);
#endif
      {
        u32x4 mf0, mf1;
        const unsigned wp = wl + L_WPHI;
        const u32x4 wp0 = lds128(wp), wp1 = lds128(wp + 1024u);
        const f32x8 o = xrecv();
        SCHED();
        pack16(ak, mf0, mf1);
        SCHED();
        f32x16 pk = MFMA(wp0, mf0, zero16());
        pk = MFMA(wp1, mf1, pk);
        {
          u32x4 tv;
          if (kh) {
#pragma unroll
            for (int i = 0; i < 4; ++i) tv[i] = pk2(av[8 + 2 * i] + o[2 * i], av[9 + 2 * i] + o[2 * i + 1]);
          } else {
#pragma unroll
            for (int i = 0; i < 4; ++i) tv[i] = pk2(av[2 * i] + o[2 * i], av[2 * i + 1] + o[2 * i + 1]);
          }
          sts128(wl + L_R0 + 49152u + (unsigned)(st * 2 + kh) * 1024u, tv);
        }
        SCHED();
        BGS(OB, NB, 4, 2);
        if (layer == 0) {
          const unsigned u0 = (unsigned)(7 * (j >> 1) + 3 * (j & 1) + 2 * kh) * 512u;
          const unsigned o = lane16 + ((kh && hh) ? ((j & 1) ? 0x40000000u : (unsigned)(7 * (j >> 1) + 5) * 512u) : u0);
          const u32x4 sent = mksent();
          st128(sent, rs_ws, o, WS_X2 + (unsigned)g * 28672u, 16);
          st128(sent, rs_ws, o, WS_O + (unsigned)g * 28672u, 16);
          st128(sent, rs_ws, o, WS_X1 + (unsigned)g * 28672u, 16);
        } else store_unit(WS_X1 + (unsigned)g * 28672u, kh, mksent());
        u32x4 id0 = lds128(wl + L_ID), id1 = lds128(lb + (lane16 ^ 256u) + L_ID);
        SCHED();
        softmax28(pk, hh, kh, rs, rs, mf0, mf1);
        asm volatile("" : "+v"(id0), "+v"(id1));
        BGS(OB, NB, 4, 3);
        f32x16 tp = MFMA(mf0, id0, zero16());
        tp = MFMA(mf1, id1, tp);
        pack16(tp, mf0, mf1);
        sts128(pk_, mf0); sts128(pk_ + 1024u, mf1);
      }
      ST();
      if (layer == 0) {
        asm volatile("s_waitcnt vmcnt(%5)" : "+v"(hc[8]), "+v"(hc[9]), "+v"(hc[10]), "+v"(hc[11]), "+v"(hx) : "n"(NA + NB + 3 + (HR == 1 ? 4 : 0)) : "memory");
        if (w >= 2 && w < 7) colsum(hc + 8, hx);
      }
      BAR();
      {
        const unsigned lo = (unsigned)((lane >> 5) * 1024 + (((lane >> 4) & 1) * 32 + (lane & 15)) * 16);
        const unsigned va = lb + L_R0 + 49152u + lo + (unsigned)((w >> 1) & 1) * 256u;
        const unsigned pa = lb + L_R0 + (unsigned)(1 - kh) * 16384u + lo + (unsigned)(w & 1) * 256u;
        f32x4 kv; kv[0] = kv[1] = kv[2] = kv[3] = 0.f;
        u32x4 fv[4], fp[4];
#pragma unroll
        for (int i = 0; i < 4; ++i) { fv[i] = lds128(va + i * 2048); fp[i] = lds128(pa + i * 4096); }
        SCHED();
#pragma unroll
        for (int i = 0; i < 4; ++i)
          kv = __builtin_amdgcn_mfma_f32_16x16x32_bf16(__builtin_bit_cast(bf16x8, fv[i]), __builtin_bit_cast(bf16x8, fp[i]), kv, 0, 0, 0);
        const unsigned gb = WS_KV + (unsigned)(layer * 16 + j) * 8192u + (unsigned)w * 1024u;
#pragma unroll
        for (int i = 0; i < 4; ++i) __builtin_amdgcn_raw_ptr_buffer_atomic_fadd_f32(kv[i], rs_ws, (unsigned)lane * 4u + i * 256, gb, 0);
      }
      SCHED();
      ST();
      const unsigned qa = wl + (unsigned)st * 4096u;
      f32x16 aq = zero16();
      if (kh) aq = MFMA(cf_frag(0, r), stf, aq);
      aq = gemm_f<14, 928>(lb + L_Q + (unsigned)(((hh ? 29 : 0) + r) * 16 + kh * (14 * 928)), xf, aq,
                           [&](auto ic) { constexpr int s = decltype(ic)::v; BGS(OC, NC, 14, s); });
      st16(qa + (kh ? L_R0 + 32768u : L_X), aq);
      if (layer == 0) asm volatile("s_waitcnt vmcnt(%0)" :: "n"(NC) : "memory"); else DRAIN();
      BAR();
      set_flag(layer, j * 16 + t);
      if (layer == 0 && tid == 64)
        __hip_atomic_store(FT(t * 16 + j), FV, __ATOMIC_RELAXED, __HIP_MEMORY_SCOPE_AGENT);
      ST();
      {
        const unsigned so = WS_WT + (unsigned)j * G_STRIDE + G_WO + (unsigned)w * 3072u;
        const unsigned ld = lb + L_R0 + (unsigned)w * 3072u;
        unsigned keep;
        asm volatile("s_mov_b32 %0, m0\n\ts_mov_b32 m0, %1\n\ts_nop 0\n\t"
                     "buffer_load_dwordx4 %2, %3, %4 offen sc1 lds\n\t"
                     "buffer_load_dwordx4 %2, %3, %4 offen offset:1024 sc1 lds\n\t"
                     "buffer_load_dwordx4 %2, %3, %4 offen offset:2048 sc1 lds\n\t"
                     "buffer_load_dwordx4 %2, %3, %4 offen offset:3072 sc1 lds\n\t"
                     "s_mov_b32 m0, %0"
                     : "=&s"(keep) : "s"(ld), "v"(lane16), "s"(rs_ws), "s"(so) : "memory");
      }
      if (HR == 0 || layer == 1) {
        const u32x4 sent = mksent();
#pragma unroll
        for (int c = 0; c < 4; ++c) store_h(c, sent);
      }
      BGS(OD, ND, 3, 0);
      {
        const unsigned pa = qa + (kh ? L_X : L_R0 + 32768u);
        if (AF & 2) add16f(pa, aq, []() {}); else add16(pa, aq);
        u32x4 qf0, qf1;
        pack16(aq, qf0, qf1);
        BGS(OD, ND, 3, 1);
        const unsigned wp = wl + L_WPHI;
        f32x16 pq = MFMA(lds128(wp), qf0, zero16());
        pq = MFMA(lds128(wp + 1024u), qf1, pq);
        SCHED();
        if (w == 0) asm volatile("global_load_dwordx2 %0, %1, off sc1" : "=v"(fbv) : "v"(p2flags()) : "memory");
        SCHED();
        softmax28(pq, hh, kh, rs, 1.0f, qf0, qf1);
        BGS(OD, ND, 3, 2);
        sts128(pa, qf0); sts128(pa + 1024u, qf1);
      }
    }
    relane();
    ST();
    if (w == 0) {
      wait_u64(fbv);
      if (__builtin_amdgcn_ballot_w64(fbv != FV) != 0ull)
      wait_flags(p2flags(), FV, 32u);
    }
    BAR();
    ST();
    if (layer == 1 && b == 0) {
      u64 e = E + 1ull;
      asm volatile("" : "+s"(e));
      if (tid == 0) __hip_atomic_store((GAS u64*)(wsg + WS_EPOCH), e, __ATOMIC_RELAXED, __HIP_MEMORY_SCOPE_AGENT);
    }
    {
      const f32x4 sa = __builtin_bit_cast(f32x4, __builtin_amdgcn_raw_buffer_load_b128(rs_ws, lane16,
          WS_KV + (unsigned)(layer * 16 + j) * 8192u + (unsigned)w * 1024u, 16));
      const unsigned qa = wl + (unsigned)st * 4096u;
      xf[0] = lds128(qa + L_R0 + 32768u); xf[1] = lds128(qa + L_R0 + 32768u + 1024u);
      xf[2] = lds128(qa + L_X); xf[3] = lds128(qa + L_X + 1024u);
      u32x2 v; v[0] = pk2(sa[0], sa[1]); v[1] = pk2(sa[2], sa[3]);
      *(LAS u32x2*)(lb + L_X + 2048u + (unsigned)(kh * 4096 + (w & 1) * 1024 +
          ((lane & 1) * 32 + ((w >> 1) & 1) * 16 + ((lane >> 2) & 3) * 4 + (lane >> 4)) * 16 + ((lane >> 1) & 1) * 8)) = v;
      DRAIN();
      ST2();
      BAR();
      ST2();
      const unsigned ka = lb + L_X + 2048u + (unsigned)(((hh ? 32 : 0) + rx) * 16);
      const u32x4 k0 = lds128(ka), k1 = lds128(ka + 1024u), k2 = lds128(ka + 4096u), k3 = lds128(ka + 5120u);
      SCHED();
      f32x16 nu = MFMA(k0, xf[0], zero16());
      nu = MFMA(k1, xf[1], nu);
      nu = MFMA(k2, xf[2], nu);
      nu = MFMA(k3, xf[3], nu);
      float den = xadd(hh ? (kh ? nu[4] : nu[12]) : 0.f);
      den = rb(rb(den) + 1e-6f);
      const float rd = HQ ? __builtin_amdgcn_rcpf(den) : 1.0f / den;
      u32x4 o0;
#pragma unroll
      for (int i = 0; i < 4; ++i) o0[i] = pk2(rb(nu[2 * i]) * rd, rb(nu[2 * i + 1]) * rd);
      store_unit(WS_O + (unsigned)g * 28672u, kh, o0);
      ST2();
      if (HR == 2 || layer == 0) {
        const u32x4 sent = mksent();
#pragma unroll
        for (int c = 0; c < 4; ++c) store_h(c, sent);
      }
      ST2();
      fill_w(IC<0>{}, 1u);
      ST2();
      BGS(OE, NE, 1, 0);
    }
    ST();
    BAR();
    relane();
    ST();
    if (layer == 0) __builtin_amdgcn_s_sleep(SL0);
    load_x14(WS_O + (unsigned)g * 28672u, IC<SL3>{});
    {
      const unsigned wc = lb + L_R0 + (unsigned)(((hh ? 28 : 0) + rx) * 16 + kh * (14 * 896));
      const unsigned vo = WS_O + (unsigned)g * 28672u + kofs;
      f32x16 a = gemm_p<14, 896>(wc, xf, zero16(),
          [&](auto ic) { refetch_x(ic, vo, SENT - 1u); }, [](auto) {});
      CST(layer * 8 + 1);
      xsend(a);
      BAR();
      const f32x8 o = xrecv();
#pragma unroll
      for (int q = 0; q < 4; ++q)
        xr[q] = pk2(bflo(xr[q]) + rb(a[2 * q] + o[2 * q]), bfhi(xr[q]) + rb(a[2 * q + 1] + o[2 * q + 1]));
      store_unit(WS_X1 + (unsigned)g * 28672u, kh, xr);
      fill_w(IC<1>{}, 0u);
      BGS(OF, NF, 1, 0);
    }
    ST();
    BAR();
    relane();
    ST();
    load_x14(WS_X1 + (unsigned)g * 28672u, IC<SL4>{});
    unsigned hb = WS_H + (unsigned)g * 114688u + kofs;
    auto ldx = [&](auto ic) { constexpr int s = decltype(ic)::v;
      xf[s % 14] = __builtin_amdgcn_raw_buffer_load_b128(rs_ws, hb, (unsigned)((s / 14) * 28672 + (s % 14) * 1024), 16); };
    {
      unsigned ww = lb + L_R0 + (unsigned)(((hh ? 32 : 0) + rx) * 16 + kh * 14336);
      asm volatile("" : "+v"(ww));
      const unsigned vo = WS_X1 + (unsigned)g * 28672u + kofs;
      float sq0 = 0.f, sq1 = 0.f, sq2 = 0.f, sq3 = 0.f, ssq = 0.f, sum = 0.f, c1 = 0.f, c2 = 0.f;
      f32x16 a, a1;
      f32x8 ap, uu;
      u32x4 wf[PD + 1];
      auto ldw = [&](auto ic) {
        constexpr int n = decltype(ic)::v, u = n / 14, s = n % 14;
        if constexpr (u < 4) wf[n % (PD + 1)] = lds128(ww + (unsigned)(((u + 1) & 1) * SLOT + s * 1024));
        else wf[n % (PD + 1)] = lds128(ww + (unsigned)(((u + 1) & 1) * SLOT + s * 896));
      };
      auto epi_item = [&](auto stc, auto elc) {
        constexpr int sg = decltype(stc)::v, i = decltype(elc)::v;
        if constexpr (sg == 0) ap[i] += uu[i];
        else if constexpr (sg == 1) uu[i] = ap[i] * ap[i];
        else if constexpr (sg == 2) uu[i] = uu[i] * c2 + c1;
        else if constexpr (sg == 3) uu[i] = uu[i] * ap[i];
        else if constexpr (sg == 4) uu[i] = __builtin_amdgcn_exp2f(uu[i]);
        else if constexpr (sg == 5) uu[i] = uu[i] * sb + sb;
        else if constexpr (sg == 6) uu[i] = __builtin_amdgcn_rcpf(uu[i]);
        else ap[i] = ap[i] * uu[i];
      };
      SCHED();
      sfor<PD>(ldw);
      SCHED();
      sfor<112>([&](auto ic) {
        constexpr int n = decltype(ic)::v, u = n / 14, s = n % 14, c = u - 1;
        f32x16& cur = (u < 4 && (u & 1)) ? a1 : a;
        f32x16& prv = (u & 1) ? a : a1;
        if constexpr (u >= 1 && u <= 6 && s == 2) {
          if constexpr (u <= 4) asm volatile("s_waitcnt lgkmcnt(1)" ::: "memory");
          BAR0();
          if constexpr (u == 1) fill_w(IC<2>{}, 1u);
          else if constexpr (u == 2) fill_w(IC<3>{}, 0u);
          else fill_c(IC<u - 3>{}, (unsigned)(u & 1));
          if constexpr (u == 1) {
            if (layer == 0) store_unit(WS_O + (unsigned)g * 28672u, kh, mksent());
            stat_recv(L_X, ssq, sum, rs, sb, stf);
            c1 = (-1.5957691216057308f * 1.4426950408889634f) * rs;
            c2 = c1 * 0.044715f * rs * rs;
          }
          if constexpr (u <= 4) prv = MFMA(cf_frag(1 + c, rx), stf, prv);
        }
        if constexpr (u >= 1 && u <= 6 && s == 9) BAR0();
        if constexpr (n == 64) CST(layer * 8 + 3);
        if constexpr (n == 80) CST(layer * 8 + 4);
        if constexpr (n == 96) CST(layer * 8 + 5);
        if constexpr (n + PD == 56) {
          relane();
          ww = lb + L_R0 + (unsigned)(((hh ? 28 : 0) + rx) * 16 + kh * (14 * 896));
          asm volatile("" : "+v"(ww));
        }
        if constexpr (n + PD < 112) ldw(IC<n + PD>{});
        if constexpr (u == 0) refetch_x(ic, vo, SENT - 1u);
        if constexpr (u >= 4) refetch(xf[s], hb, (unsigned)((u - 4) * 28672 + s * 1024), SENT - 1u);
        if constexpr (s == 0 && u <= 4) cur = MFMA(wf[n % (PD + 1)], xf[s], zero16());
        else cur = MFMA(wf[n % (PD + 1)], xf[s], cur);
        if constexpr (u == 0) { if (Q4 && (s & 1)) ssq_step(xf[s], sq2, sq3); else ssq_step(xf[s], sq0, sq1); }
        if constexpr (u >= 1 && u <= 4) {
          if constexpr (s == 0) {
            if constexpr (u == 1) {
              CST(layer * 8 + 2);
              ssq = xadd(Q4 ? (sq0 + sq1) + (sq2 + sq3) : sq0 + sq1); sum = xadd(hh ? 0.f : (kh ? prv[0] : prv[8]));
              stat_send(L_X, ssq, sum);
              if (kh) xsend(prv);
            } else xsend(prv);
            ST();
          }
          if constexpr (s == 3) {
#pragma unroll
            for (int i = 0; i < 8; ++i) ap[i] = prv[i];
            uu = xrecv();
          }
          if constexpr (s >= 4 && s < 12) {
            if (u != 1 || !kh) sfor<8>([&](auto kc) { epi_item(IC<s - 4>{}, kc); });
          }
          if constexpr (s == 12) {
            u32x4 f0;
#pragma unroll
            for (int i = 0; i < 4; ++i) f0[i] = pk2(ap[2 * i], ap[2 * i + 1]);
            store_h((c + 3) & 3, f0);
          }
        }
        if constexpr (u >= 3 && n + 14 < 112) ldx(IC<n - 42>{});
        if constexpr (u == 1) BGS(OG, NG, 14, s);
        if constexpr (u == 2 && s < 12) BGS(OH, NH, 12, s);
        SCHED();
      });
      CST(layer * 8 + 6);
      xsend(a);
      DRAIN();
      BAR();
      const f32x8 o = xrecv();
      f32x8 bb;
#pragma unroll
      for (int q = 0; q < 2; ++q) {
        const f32x4 v = ldsf4(lb + L_B2 + (unsigned)(kh * 64 + q * 32) + (hh ? 16u : 0u));
        bb[4 * q] = v[0]; bb[4 * q + 1] = v[1]; bb[4 * q + 2] = v[2]; bb[4 * q + 3] = v[3];
      }
#pragma unroll
      for (int q = 0; q < 4; ++q)
        xr[q] = pk2(bflo(xr[q]) + rb(rb(a[2 * q] + o[2 * q]) + bb[2 * q]), bfhi(xr[q]) + rb(rb(a[2 * q + 1] + o[2 * q + 1]) + bb[2 * q + 1]));
    }
    ST();
    if (layer == 0) {
      store_unit(WS_X2 + (unsigned)g * 28672u, kh, xr);
      BAR();
    } else {
      const unsigned rowb = (unsigned)(((g * 32 + r) * 448 + 28 * j + (hh ? 4 : 0) + 16 * kh) * 2);
      u32x2 a, c; a[0] = xr[0]; a[1] = xr[1]; c[0] = xr[2]; c[1] = xr[3];
      __builtin_amdgcn_raw_buffer_store_b64(a, rs_out, rowb, 0, 0);
      if (kh == 0 || !hh) __builtin_amdgcn_raw_buffer_store_b64(c, rs_out, rowb + 16, 0, 0);
    }
    ST();
  }
  asm volatile(".Lfwd_z:");
  RT(1);
}

#if defined(__HIP_DEVICE_COMPILE__)
#pragma clang attribute pop
#endif

extern "C" void kernel_launch(void* const* d_in, const int* in_sizes, int n_in,
                              void* d_out, int out_size, void* d_ws, size_t ws_size,
                              hipStream_t stream) {
  Params p;
  p.x = d_in[0]; p.wqkv = d_in[1]; p.wphi = d_in[2]; p.wo = d_in[3];
  p.w1 = d_in[5]; p.b1 = d_in[6]; p.w2 = d_in[7]; p.b2 = d_in[8];
  p.out = d_out; p.ws = d_ws;
  fwd<<<256, 512, 0, stream>>>(p);
}
```
